```python
import torch

def _softmax(x, axis=-1):
    return torch.softmax(x, dim=axis)
def _gelu(x):
    return torch.nn.functional.gelu(x, approximate="tanh")
def _silu(x):
    return torch.nn.functional.silu(x)
def _sigmoid(x):
    return torch.sigmoid(x)
def _layernorm(x, axis=-1, eps=1e-5):
    m = x.mean(dim=axis, keepdim=True)
    v = x.var(dim=axis, keepdim=True, unbiased=False)
    return (x - m) / torch.sqrt(v + eps)
def _scatter_add(tgt, idx, src, axis=0):
    return tgt.clone().index_add_(axis, idx, src)
def _segment_sum(src, seg, n):
    out = torch.zeros((n,) + src.shape[1:], dtype=src.dtype)
    return out.index_add_(0, seg, src)
def _pad(x, pw):
    flat = tuple(v for pair in reversed(pw) for v in pair)
    return torch.nn.functional.pad(x, flat)

kernel_name = 'concat_heads__grouped_kv_mqa__compressed_kv_cla__prompt'

def setup_inputs():

    _w = (lambda a: (lambda t: t.to(torch.bfloat16) if t.is_floating_point() else t)(torch.as_tensor(a)))
    def _A_setup():
        B, S, D, H = 1, 2048, 512, 32
        Dh = D // H
        return {
            "x": _w((lambda *s: torch.randn(*s, dtype=torch.bfloat16) * 0.05)(B, S, D)),
            "wq": _w((lambda *s: torch.randn(*s, dtype=torch.bfloat16) * 0.8)(D, H * Dh)),
            "wk": _w((lambda *s: torch.randn(*s, dtype=torch.bfloat16) * 0.8)(D, Dh)),
            "wv": _w((lambda *s: torch.randn(*s, dtype=torch.bfloat16) * 0.05)(D, Dh)),
            "wo": _w((lambda *s: torch.randn(*s, dtype=torch.bfloat16) * 0.05)(H * Dh, D)),
        }
    _A_in = _A_setup()

    def _B_setup():
        B, H, Sq, Sk, D, M = 1, 32, 2048, 2048, 128, 512
        Dc = 4 * D
        return {
            "x": _w((lambda *s: torch.randn(*s, dtype=torch.bfloat16) * 3.2)(B, Sq, M)),
            "c_kv": _w((lambda *s: torch.randn(*s, dtype=torch.bfloat16) * 0.1)(B, Sk, Dc)),
            "w_q": _w((lambda *s: torch.randn(*s, dtype=torch.bfloat16) * 6.4)(M, H, D) * (M ** -0.5)),
            "w_uk": _w((lambda *s: torch.randn(*s, dtype=torch.bfloat16) * 6.4)(Dc, H, D) * (Dc ** -0.5)),
            "w_uv": _w((lambda *s: torch.randn(*s, dtype=torch.bfloat16) * 0.1)(Dc, H, D) * (Dc ** -0.5)),
            "w_o": _w((lambda *s: torch.randn(*s, dtype=torch.bfloat16) * 0.1)(H, D, M) * ((H * D) ** -0.5)),
        }
    _B_in = _B_setup()

    return {
        "A_x": _A_in["x"],
        "A_wq": _A_in["wq"],
        "A_wk": _A_in["wk"],
        "A_wv": _A_in["wv"],
        "A_wo": _A_in["wo"],
        "B_x": _B_in["x"],
        "B_c_kv": _B_in["c_kv"],
        "B_w_q": _B_in["w_q"],
        "B_w_uk": _B_in["w_uk"],
        "B_w_uv": _B_in["w_uv"],
        "B_w_o": _B_in["w_o"],
    }

def reference(A_x, A_wq, A_wk, A_wv, A_wo, B_x, B_c_kv, B_w_q, B_w_uk, B_w_uv, B_w_o):
    def _A_ref(x, wq, wk, wv, wo):
        B, S, D = x.shape
        H = 32
        Dh = D // H
        q = (lambda x, *d: x.permute(*d))((x @ wq).reshape(B, S, H, Dh), 0, 2, 1, 3)
        k = x @ wk
        v = x @ wv
        scores = torch.einsum("bhqd,bkd->bhqk", q, k) * (Dh ** -0.5)
        p = _softmax(scores, axis=-1)
        o = torch.einsum("bhqk,bkd->bhqd", p, v)
        return (lambda x, *d: x.permute(*d))(o, 0, 2, 1, 3).reshape(B, S, H * Dh) @ wo

    def _B_ref(x, c_kv, w_q, w_uk, w_uv, w_o):
        B, Sq, M = x.shape[0], x.shape[1], x.shape[2]
        Sk = c_kv.shape[1]
        H, D = w_q.shape[1], w_q.shape[2]
        q = torch.einsum("bqm,mhd->bhqd", x, w_q)
        k = torch.einsum("bkc,chd->bhkd", c_kv, w_uk)
        v = torch.einsum("bkc,chd->bhkd", c_kv, w_uv)
        scores = torch.einsum("bhqd,bhkd->bhqk", q, k) * (D ** -0.5)
        off = Sk - Sq
        qi = torch.arange(Sq)[:, None]
        kj = torch.arange(Sk)[None, :]
        cmask = kj <= (qi + off)
        scores = torch.where(cmask[None, None], scores, -1e9)
        p = _softmax(scores, axis=-1)
        ctx = torch.einsum("bhqk,bhkd->bhqd", p, v)
        return torch.einsum("bhqd,hdm->bqm", ctx, w_o)

    a = _A_ref(A_x, A_wq, A_wk, A_wv, A_wo)
    b = _B_ref(B_x, B_c_kv, B_w_q, B_w_uk, B_w_uv, B_w_o)
    return torch.concatenate([a, b], axis=-1)
```

```cpp
#include <hip/hip_runtime.h>
#include <hip/hip_bf16.h>
#include <stdint.h>

typedef short bf16x8 __attribute__((ext_vector_type(8)));
typedef short s16x4 __attribute__((ext_vector_type(4)));
typedef float f32x16 __attribute__((ext_vector_type(16)));
typedef float f32x4 __attribute__((ext_vector_type(4)));
typedef unsigned u32x4 __attribute__((ext_vector_type(4)));
typedef unsigned u32x2 __attribute__((ext_vector_type(2)));
typedef unsigned short u16;
typedef __bf16 bf2_t __attribute__((ext_vector_type(2)));
typedef __attribute__((address_space(3))) s16x4* lds_s16x4_p;

constexpr int S = 2048;
constexpr float LOG2E = 1.4426950408889634f;

__device__ __forceinline__ unsigned cvtpk(float lo, float hi) {
    bf2_t v = {(__bf16)lo, (__bf16)hi}; return __builtin_bit_cast(unsigned, v);
}
__device__ __forceinline__ s16x4 trread(const char* p) {
    return __builtin_amdgcn_ds_read_tr16_b64_v4i16((lds_s16x4_p)(p));
}
__device__ __forceinline__ bf16x8 cat8(s16x4 a, s16x4 b) {
    return (bf16x8){a[0], a[1], a[2], a[3], b[0], b[1], b[2], b[3]};
}
__device__ __forceinline__ int swz256(int row) { return ((row & 3) << 2) | ((row >> 2) & 3); }
__device__ __forceinline__ int off_b(int row, int ch) { return 256 * row + 16 * (ch ^ swz256(row)); }
__device__ __forceinline__ void glds16(const void* g, char* l) {
    __builtin_amdgcn_global_load_lds((const unsigned*)g, (unsigned*)l, 16, 0, 0);
}
#define WAITV0() asm volatile("s_waitcnt vmcnt(0)" ::: "memory")
__device__ __forceinline__ float swapmax(float v) {
    auto rr = __builtin_amdgcn_permlane32_swap(__float_as_uint(v), __float_as_uint(v), false, false);
    return fmaxf(__uint_as_float(rr[0]), __uint_as_float(rr[1]));
}
__device__ __forceinline__ float swapsum(float v) {
    auto rr = __builtin_amdgcn_permlane32_swap(__float_as_uint(v), __float_as_uint(v), false, false);
    return __uint_as_float(rr[0]) + __uint_as_float(rr[1]);
}

constexpr int G_STAGE = 32768;
constexpr int G_LDS = 2 * G_STAGE;

template <bool KVA>
__device__ __forceinline__ void gemm_stage(char* buf, const u16* A, int lda, const u16* W, int ldw, const u16* W2,
                                           int m0, int n0, int k0, int tid) {
#pragma unroll
    for (int i = 0; i < 4; ++i) {
        int p = tid + 256 * i;
        int row = p >> 3, slot = p & 7, ch = slot ^ ((row >> 1) & 7);
        glds16(A + (size_t)(m0 + row) * lda + k0 + ch * 8, buf + p * 16);
    }
#pragma unroll
    for (int i = 0; i < 4; ++i) {
        int p = tid + 256 * i;
        int row = p >> 4, slot = p & 15, ch = slot ^ swz256(row);
        const u16* src;
        if (KVA) {
            src = (ch < 2) ? W + (size_t)(k0 + row) * 16 + ch * 8
                           : ((ch < 4) ? W2 + (size_t)(k0 + row) * 16 + (ch - 2) * 8 : W + (size_t)(k0 + row) * 16);
        } else {
            src = W + (size_t)(k0 + row) * ldw + n0 + ch * 8;
        }
        glds16(src, buf + 16384 + p * 16);
    }
}

template <bool KVA>
__device__ __forceinline__ void gemm_tile(char* lds, const u16* A, int lda, const u16* W, int ldw, const u16* W2,
                                          int m0, int n0, int kbeg, int nkt, f32x16 (&acc)[2][2]) {
    const int tid = threadIdx.x, lane = tid & 63, wid = tid >> 6, wr = wid >> 1, wc = wid & 1;
    const int r32 = lane & 31, hi = lane >> 5;
#pragma unroll
    for (int i = 0; i < 2; ++i)
#pragma unroll
        for (int j = 0; j < 2; ++j) acc[i][j] = f32x16{};
    gemm_stage<KVA>(lds, A, lda, W, ldw, W2, m0, n0, kbeg, tid);
    int aoff[2];
#pragma unroll
    for (int mi = 0; mi < 2; ++mi) { int row = wr * 64 + mi * 32 + r32; aoff[mi] = 128 * row; }
    const int asw = ((wr * 64 + r32) >> 1) & 7;
    const int blk = (lane >> 4) & 1, q4 = (lane & 15) >> 2, p4 = lane & 3;
    for (int t = 0; t < nkt; ++t) {
        WAITV0();
        __syncthreads();
        char* cur = lds + (t & 1) * G_STAGE;
        if (t + 1 < nkt) gemm_stage<KVA>(lds + ((t + 1) & 1) * G_STAGE, A, lda, W, ldw, W2, m0, n0, kbeg + (t + 1) * 64, tid);
#pragma unroll
        for (int ks = 0; ks < 4; ++ks) {
            bf16x8 xf[2], wf[2];
#pragma unroll
            for (int mi = 0; mi < 2; ++mi) xf[mi] = *(const bf16x8*)(cur + aoff[mi] + 16 * ((2 * ks + hi) ^ asw));
#pragma unroll
            for (int ni = 0; ni < 2; ++ni) {
                int c = wc * 2 + ni;
                int row0 = 16 * ks + 8 * hi + q4;
                s16x4 lo = trread(cur + 16384 + off_b(row0, 4 * c + 2 * blk + (p4 >> 1)) + 8 * (p4 & 1));
                s16x4 hh = trread(cur + 16384 + off_b(row0 + 4, 4 * c + 2 * blk + (p4 >> 1)) + 8 * (p4 & 1));
                wf[ni] = cat8(lo, hh);
            }
#pragma unroll
            for (int mi = 0; mi < 2; ++mi)
#pragma unroll
                for (int ni = 0; ni < 2; ++ni)
                    acc[mi][ni] = __builtin_amdgcn_mfma_f32_32x32x16_bf16(wf[ni], xf[mi], acc[mi][ni], 0, 0, 0);
        }
    }
}

__device__ __forceinline__ void store_bf16_tile(u16* dst, int ldd, f32x16 (&acc)[2][2]) {
    const int tid = threadIdx.x, lane = tid & 63, wid = tid >> 6, wr = wid >> 1, wc = wid & 1;
    const int r32 = lane & 31, hi = lane >> 5;
#pragma unroll
    for (int mi = 0; mi < 2; ++mi)
#pragma unroll
        for (int ni = 0; ni < 2; ++ni)
#pragma unroll
            for (int g = 0; g < 4; ++g) {
                u32x2 v = {cvtpk(acc[mi][ni][4 * g], acc[mi][ni][4 * g + 1]), cvtpk(acc[mi][ni][4 * g + 2], acc[mi][ni][4 * g + 3])};
                *(u32x2*)(dst + (size_t)(wr * 64 + mi * 32 + r32) * ldd + wc * 64 + ni * 32 + 8 * g + 4 * hi) = v;
            }
}

__global__ __launch_bounds__(256, 2) void proj_kernel(const u16* __restrict__ xa, const u16* __restrict__ wqa, const u16* __restrict__ wka,
                                                      const u16* __restrict__ wva, const u16* __restrict__ xb, const u16* __restrict__ ckv,
                                                      const u16* __restrict__ wq, const u16* __restrict__ wuk, const u16* __restrict__ wuv,
                                                      u16* __restrict__ QKV, u16* __restrict__ Qa, u16* __restrict__ Ka, u16* __restrict__ Vimg) {
    extern __shared__ __attribute__((aligned(16))) char lds[];
    const int b = blockIdx.x;
    const int T = (b & 7) * 202 + (b >> 3);
    f32x16 acc[2][2];
    if (T < 1536) {
        const int pj = T >> 9, nt = (T & 511) >> 4, mt = T & 15;
        const u16* A = pj == 0 ? xb : ckv;
        const u16* W = pj == 0 ? wq : (pj == 1 ? wuk : wuv);
        gemm_tile<false>(lds, A, 512, W, 4096, W, mt * 128, nt * 128, 0, 8, acc);
        store_bf16_tile(QKV + (size_t)pj * (32 * S * 128) + ((size_t)nt * S + mt * 128) * 128, 128, acc);
    } else if (T < 1600) {
        const int u = T - 1536, nt = u >> 4, mt = u & 15;
        gemm_tile<false>(lds, xa, 512, wqa, 512, wqa, mt * 128, nt * 128, 0, 8, acc);
        store_bf16_tile(Qa + (size_t)(mt * 128) * 512 + nt * 128, 512, acc);
    } else {
        const int mt = T - 1600;
        gemm_tile<true>(lds, xa, 512, wka, 16, wva, mt * 128, 0, 0, 8, acc);
        const int tid = threadIdx.x, lane = tid & 63, wid = tid >> 6, wr = wid >> 1, wc = wid & 1;
        const int r32 = lane & 31, hi = lane >> 5;
        if (wc == 0) {
#pragma unroll
            for (int mi = 0; mi < 2; ++mi) {
                const int m = mt * 128 + wr * 64 + mi * 32 + r32;
#pragma unroll
                for (int g = 0; g < 2; ++g) {
                    u32x2 v = {cvtpk(acc[mi][0][4 * g], acc[mi][0][4 * g + 1]), cvtpk(acc[mi][0][4 * g + 2], acc[mi][0][4 * g + 3])};
                    *(u32x2*)(Ka + (size_t)m * 16 + 8 * g + 4 * hi) = v;
                }
                const int s = m >> 4, kk = m & 15, hh = (kk >> 2) & 1, j = (kk & 3) + 4 * (kk >> 3);
#pragma unroll
                for (int r = 8; r < 16; ++r) {
                    const int d = (r & 3) + 8 * ((r >> 2) - 2) + 4 * hi;
                    Vimg[((s * 17 + d) * 2 + hh) * 8 + j] = (u16)(cvtpk(acc[mi][0][r], 0.f) & 0xffff);
                }
                if (hi == 0) Vimg[((s * 17 + 16) * 2 + hh) * 8 + j] = (u16)0x3F80;
            }
        }
    }
}

__global__ __launch_bounds__(256, 2) void oproj_kernel(const u16* __restrict__ ctxB, const u16* __restrict__ wo_b,
                                                       const u16* __restrict__ ctxA, const u16* __restrict__ wo_a,
                                                       float* __restrict__ part, u16* __restrict__ out) {
    extern __shared__ __attribute__((aligned(16))) char lds[];
    const int b = blockIdx.x;
    f32x16 acc[2][2];
    if (b < 256) {
        const int sp = b & 3, nt = (b >> 2) & 3, mt = b >> 4;
        gemm_tile<false>(lds, ctxB, 4096, wo_b, 512, wo_b, mt * 128, nt * 128, sp * 1024, 16, acc);
        const int tid = threadIdx.x, lane = tid & 63, wid = tid >> 6, wr = wid >> 1, wc = wid & 1;
        const int r32 = lane & 31, hi = lane >> 5;
        float* dst = part + (size_t)sp * S * 512;
#pragma unroll
        for (int mi = 0; mi < 2; ++mi)
#pragma unroll
            for (int ni = 0; ni < 2; ++ni)
#pragma unroll
                for (int g = 0; g < 4; ++g) {
                    f32x4 v = {acc[mi][ni][4 * g], acc[mi][ni][4 * g + 1], acc[mi][ni][4 * g + 2], acc[mi][ni][4 * g + 3]};
                    *(f32x4*)(dst + (size_t)(mt * 128 + wr * 64 + mi * 32 + r32) * 512 + nt * 128 + wc * 64 + ni * 32 + 8 * g + 4 * hi) = v;
                }
    } else {
        const int u = b - 256, nt = u & 3, mt = u >> 2;
        gemm_tile<false>(lds, ctxA, 512, wo_a, 512, wo_a, mt * 128, nt * 128, 0, 8, acc);
        store_bf16_tile(out + (size_t)(mt * 128) * 1024 + nt * 128, 1024, acc);
    }
}

__global__ __launch_bounds__(256) void reduce_kernel(const float* __restrict__ part, u16* __restrict__ out) {
    const int i = blockIdx.x * 256 + threadIdx.x;
    const f32x4* p = (const f32x4*)part;
    f32x4 a = p[i], b2 = p[i + 262144], c = p[i + 2 * 262144], d = p[i + 3 * 262144];
    f32x4 s = (a + b2) + (c + d);
    const int m = i >> 7, n = (i & 127) * 4;
    u32x2 v = {cvtpk(s[0], s[1]), cvtpk(s[2], s[3])};
    *(u32x2*)(out + (size_t)m * 1024 + 512 + n) = v;
}

constexpr int AB_TILE = 65536;
constexpr int AB_LDS = 2 * AB_TILE;

__device__ __forceinline__ void ab_stage(char* buf, const u16* Kh, const u16* Vh, int t, int tid) {
#pragma unroll
    for (int i = 0; i < 4; ++i) {
        int p = tid + 512 * i;
        int row = p >> 4, ch = (p & 15) ^ swz256(row);
        glds16(Kh + (size_t)(128 * t + row) * 128 + ch * 8, buf + p * 16);
    }
#pragma unroll
    for (int i = 0; i < 4; ++i) {
        int p = tid + 512 * i;
        int row = p >> 4, ch = (p & 15) ^ swz256(row);
        glds16(Vh + (size_t)(128 * t + row) * 128 + ch * 8, buf + 32768 + p * 16);
    }
}

__global__ __launch_bounds__(512, 1) void attnB_kernel(const u16* __restrict__ QKV, u16* __restrict__ ctx) {
    extern __shared__ __attribute__((aligned(16))) char lds[];
    const int tid = threadIdx.x, lane = tid & 63, wid = __builtin_amdgcn_readfirstlane(tid >> 6);
    const int rg = wid & 3, kh = wid >> 2, r32 = lane & 31, hi = lane >> 5;
    const int b = blockIdx.x, jj = b >> 3, head = (b & 7) * 4 + (jj >> 3), x = jj & 7;
    const u16* Qh = QKV + (size_t)head * S * 128;
    const u16* Kh = QKV + (size_t)32 * S * 128 + (size_t)head * S * 128;
    const u16* Vh = QKV + (size_t)64 * S * 128 + (size_t)head * S * 128;
    constexpr float C2 = 0.08838834764831845f * LOG2E;
    const int blk = (lane >> 4) & 1, q4 = (lane & 15) >> 2, p4 = lane & 3;
    for (int pass = 0; pass < 2; ++pass) {
        const int qb = pass == 0 ? 15 - x : x;
        const int nt = qb + 1;
        ab_stage(lds, Kh, Vh, 0, tid);
        bf16x8 qf[8];
#pragma unroll
        for (int d0 = 0; d0 < 8; ++d0) qf[d0] = *(const bf16x8*)(Qh + (size_t)(128 * qb + 32 * rg + r32) * 128 + 16 * d0 + 8 * hi);
        f32x16 o[4];
#pragma unroll
        for (int i = 0; i < 4; ++i) o[i] = f32x16{};
        float m = -1e30f, l = 0.f;
        for (int t = 0; t < nt; ++t) {
            WAITV0();
            __syncthreads();
            const char* cur = lds + (t & 1) * AB_TILE;
            if (t + 1 < nt) ab_stage(lds + ((t + 1) & 1) * AB_TILE, Kh, Vh, t + 1, tid);
            f32x16 s0 = f32x16{}, s1 = f32x16{};
#pragma unroll
            for (int d0 = 0; d0 < 8; ++d0) {
                bf16x8 k0 = *(const bf16x8*)(cur + off_b(64 * kh + r32, 2 * d0 + hi));
                bf16x8 k1 = *(const bf16x8*)(cur + off_b(64 * kh + 32 + r32, 2 * d0 + hi));
                s0 = __builtin_amdgcn_mfma_f32_32x32x16_bf16(k0, qf[d0], s0, 0, 0, 0);
                s1 = __builtin_amdgcn_mfma_f32_32x32x16_bf16(k1, qf[d0], s1, 0, 0, 0);
            }
            if (t == nt - 1) {
                const int dq = 32 * rg + r32 - 64 * kh - 4 * hi;
                const float NEG = -__builtin_inff();
#pragma unroll
                for (int r = 0; r < 16; ++r) {
                    const int c = (r & 3) + 8 * (r >> 2);
                    if (c > dq) s0[r] = NEG;
                    if (c + 32 > dq) s1[r] = NEG;
                }
            }
            float pm = s0[0];
#pragma unroll
            for (int r = 1; r < 16; ++r) pm = fmaxf(pm, s0[r]);
#pragma unroll
            for (int r = 0; r < 16; ++r) pm = fmaxf(pm, s1[r]);
            pm = swapmax(pm);
            if (!__all(pm <= m)) {
                const float mn = fmaxf(m, pm);
                const float alpha = __builtin_amdgcn_exp2f((m - mn) * C2);
                m = mn; l *= alpha;
#pragma unroll
                for (int i = 0; i < 4; ++i)
#pragma unroll
                    for (int r = 0; r < 16; ++r) o[i][r] *= alpha;
            }
            const float mc = -m * C2;
            float ps = 0.f;
#pragma unroll
            for (int r = 0; r < 16; ++r) { s0[r] = __builtin_amdgcn_exp2f(fmaf(s0[r], C2, mc)); ps += s0[r]; }
#pragma unroll
            for (int r = 0; r < 16; ++r) { s1[r] = __builtin_amdgcn_exp2f(fmaf(s1[r], C2, mc)); ps += s1[r]; }
            l += ps;
            bf16x8 pf[4];
#pragma unroll
            for (int s = 0; s < 2; ++s) {
                u32x4 w0 = {cvtpk(s0[8 * s], s0[8 * s + 1]), cvtpk(s0[8 * s + 2], s0[8 * s + 3]), cvtpk(s0[8 * s + 4], s0[8 * s + 5]), cvtpk(s0[8 * s + 6], s0[8 * s + 7])};
                u32x4 w1 = {cvtpk(s1[8 * s], s1[8 * s + 1]), cvtpk(s1[8 * s + 2], s1[8 * s + 3]), cvtpk(s1[8 * s + 4], s1[8 * s + 5]), cvtpk(s1[8 * s + 6], s1[8 * s + 7])};
                pf[s] = *(bf16x8*)&w0; pf[2 + s] = *(bf16x8*)&w1;
            }
            const char* vb = cur + 32768;
#pragma unroll
            for (int ks = 0; ks < 4; ++ks) {
                const int row0 = 64 * kh + 16 * ks + 4 * hi + q4;
#pragma unroll
                for (int dt = 0; dt < 4; ++dt) {
                    s16x4 lo = trread(vb + off_b(row0, 4 * dt + 2 * blk + (p4 >> 1)) + 8 * (p4 & 1));
                    s16x4 hh = trread(vb + off_b(row0 + 8, 4 * dt + 2 * blk + (p4 >> 1)) + 8 * (p4 & 1));
                    o[dt] = __builtin_amdgcn_mfma_f32_32x32x16_bf16(cat8(lo, hh), pf[ks], o[dt], 0, 0, 0);
                }
            }
        }
        l = swapsum(l);
        __syncthreads();
        float* mo = (float*)lds;
        float* ml = (float*)(lds + 65536);
        if (kh == 1) {
#pragma unroll
            for (int i = 0; i < 4; ++i)
#pragma unroll
                for (int r = 0; r < 16; ++r) mo[(rg * 64 + i * 16 + r) * 64 + lane] = o[i][r];
            ml[(rg * 2) * 64 + lane] = m; ml[(rg * 2 + 1) * 64 + lane] = l;
        }
        __syncthreads();
        if (kh == 0) {
            const float m2 = ml[(rg * 2) * 64 + lane], l2 = ml[(rg * 2 + 1) * 64 + lane];
            const float mn = fmaxf(m, m2);
            const float a1 = __builtin_amdgcn_exp2f((m - mn) * C2), a2 = __builtin_amdgcn_exp2f((m2 - mn) * C2);
            const float inv = 1.f / (l * a1 + l2 * a2);
            const float f1 = a1 * inv, f2 = a2 * inv;
            u16* dst = ctx + (size_t)(128 * qb + 32 * rg + r32) * 4096 + head * 128;
#pragma unroll
            for (int i = 0; i < 4; ++i)
#pragma unroll
                for (int g = 0; g < 4; ++g) {
                    float v[4];
#pragma unroll
                    for (int e = 0; e < 4; ++e) v[e] = o[i][4 * g + e] * f1 + mo[(rg * 64 + i * 16 + 4 * g + e) * 64 + lane] * f2;
                    u32x2 w = {cvtpk(v[0], v[1]), cvtpk(v[2], v[3])};
                    *(u32x2*)(dst + 32 * i + 8 * g + 4 * hi) = w;
                }
        }
        __syncthreads();
    }
}

constexpr int AA_LDS = 65536 + 69632;
__global__ __launch_bounds__(512, 1) void attnA_kernel(const u16* __restrict__ Qa, const u16* __restrict__ Ka,
                                                       const u16* __restrict__ Vimg, u16* __restrict__ ctxA) {
    extern __shared__ __attribute__((aligned(16))) char lds[];
    const int tid = threadIdx.x, lane = tid & 63, wid = __builtin_amdgcn_readfirstlane(tid >> 6);
    const int r32 = lane & 31, hi = lane >> 5;
#pragma unroll
    for (int i = 0; i < 8; ++i) { int p = tid + 512 * i; glds16(Ka + p * 8, lds + p * 16); }
#pragma unroll
    for (int i = 0; i < 9; ++i) { int p = tid + 512 * i; if (p < 4352) glds16(Vimg + p * 8, lds + 65536 + p * 16); }
    const int g = blockIdx.x * 8 + wid, head = g & 31, qblk = g >> 5;
    const int row = 32 * qblk + r32;
    const bf16x8 qf = *(const bf16x8*)(Qa + (size_t)row * 512 + head * 16 + 8 * hi);
    constexpr float C2 = 0.25f * LOG2E;
    const int dd = r32 <= 16 ? r32 : (r32 & 15);
    const char* kp = lds + r32 * 32 + 16 * hi;
    const char* vp = lds + 65536 + (dd * 2 + hi) * 16;
    f32x16 o = f32x16{};
    float m = -1e30f;
    WAITV0();
    __syncthreads();
    for (int it = 0; it < 32; ++it) {
        bf16x8 k0 = *(const bf16x8*)(kp + it * 2048);
        bf16x8 k1 = *(const bf16x8*)(kp + it * 2048 + 1024);
        f32x16 s0 = __builtin_amdgcn_mfma_f32_32x32x16_bf16(k0, qf, f32x16{}, 0, 0, 0);
        f32x16 s1 = __builtin_amdgcn_mfma_f32_32x32x16_bf16(k1, qf, f32x16{}, 0, 0, 0);
        float pm = s0[0];
#pragma unroll
        for (int r = 1; r < 16; ++r) pm = fmaxf(pm, s0[r]);
#pragma unroll
        for (int r = 0; r < 16; ++r) pm = fmaxf(pm, s1[r]);
        pm = swapmax(pm);
        if (!__all(pm <= m)) {
            const float mn = fmaxf(m, pm);
            const float alpha = __builtin_amdgcn_exp2f((m - mn) * C2);
            m = mn;
#pragma unroll
            for (int r = 0; r < 9; ++r) o[r] *= alpha;
        }
        const float mc = -m * C2;
#pragma unroll
        for (int r = 0; r < 16; ++r) s0[r] = __builtin_amdgcn_exp2f(fmaf(s0[r], C2, mc));
#pragma unroll
        for (int r = 0; r < 16; ++r) s1[r] = __builtin_amdgcn_exp2f(fmaf(s1[r], C2, mc));
#pragma unroll
        for (int s = 0; s < 2; ++s) {
            u32x4 w0 = {cvtpk(s0[8 * s], s0[8 * s + 1]), cvtpk(s0[8 * s + 2], s0[8 * s + 3]), cvtpk(s0[8 * s + 4], s0[8 * s + 5]), cvtpk(s0[8 * s + 6], s0[8 * s + 7])};
            u32x4 w1 = {cvtpk(s1[8 * s], s1[8 * s + 1]), cvtpk(s1[8 * s + 2], s1[8 * s + 3]), cvtpk(s1[8 * s + 4], s1[8 * s + 5]), cvtpk(s1[8 * s + 6], s1[8 * s + 7])};
            bf16x8 v0 = *(const bf16x8*)(vp + (4 * it + s) * 544);
            bf16x8 v1 = *(const bf16x8*)(vp + (4 * it + 2 + s) * 544);
            o = __builtin_amdgcn_mfma_f32_32x32x16_bf16(v0, *(bf16x8*)&w0, o, 0, 0, 0);
            o = __builtin_amdgcn_mfma_f32_32x32x16_bf16(v1, *(bf16x8*)&w1, o, 0, 0, 0);
        }
    }
    const float l = __shfl(o[8], r32);
    const float inv = 1.f / l;
    u16* dst = ctxA + (size_t)row * 512 + head * 16 + 4 * hi;
#pragma unroll
    for (int g2 = 0; g2 < 2; ++g2) {
        u32x2 w = {cvtpk(o[4 * g2] * inv, o[4 * g2 + 1] * inv), cvtpk(o[4 * g2 + 2] * inv, o[4 * g2 + 3] * inv)};
        *(u32x2*)(dst + 8 * g2) = w;
    }
}

extern "C" void kernel_launch(void* const* d_in, const int* in_sizes, int n_in,
                              void* d_out, int out_size, void* d_ws, size_t ws_size,
                              hipStream_t stream) {
    const u16* A_x = (const u16*)d_in[0];
    const u16* A_wq = (const u16*)d_in[1];
    const u16* A_wk = (const u16*)d_in[2];
    const u16* A_wv = (const u16*)d_in[3];
    const u16* A_wo = (const u16*)d_in[4];
    const u16* B_x = (const u16*)d_in[5];
    const u16* B_ckv = (const u16*)d_in[6];
    const u16* B_wq = (const u16*)d_in[7];
    const u16* B_wuk = (const u16*)d_in[8];
    const u16* B_wuv = (const u16*)d_in[9];
    const u16* B_wo = (const u16*)d_in[10];
    char* ws = (char*)d_ws;
    const size_t MB = 1 << 20;
    u16* QKV = (u16*)ws;
    u16* ctxB = (u16*)(ws + 48 * MB);
    u16* Qa = (u16*)(ws + 64 * MB);
    u16* ctxA = (u16*)(ws + 66 * MB);
    u16* Ka = (u16*)(ws + 68 * MB);
    u16* Vimg = (u16*)(ws + 68 * MB + 65536);
    float* part = (float*)(ws + 72 * MB);
    u16* out = (u16*)d_out;

    hipFuncSetAttribute((const void*)proj_kernel, hipFuncAttributeMaxDynamicSharedMemorySize, G_LDS);
    hipFuncSetAttribute((const void*)oproj_kernel, hipFuncAttributeMaxDynamicSharedMemorySize, G_LDS);
    hipFuncSetAttribute((const void*)attnB_kernel, hipFuncAttributeMaxDynamicSharedMemorySize, AB_LDS);
    hipFuncSetAttribute((const void*)attnA_kernel, hipFuncAttributeMaxDynamicSharedMemorySize, AA_LDS);

    proj_kernel<<<1616, 256, G_LDS, stream>>>(A_x, A_wq, A_wk, A_wv, B_x, B_ckv, B_wq, B_wuk, B_wuv, QKV, Qa, Ka, Vimg);
    attnB_kernel<<<256, 512, AB_LDS, stream>>>(QKV, ctxB);
    attnA_kernel<<<256, 512, AA_LDS, stream>>>(Qa, Ka, Vimg, ctxA);
    oproj_kernel<<<320, 256, G_LDS, stream>>>(ctxB, B_wo, ctxA, A_wo, part, out);
    reduce_kernel<<<1024, 256, 0, stream>>>(part, out);
}
```

```cpp
#include <hip/hip_runtime.h>
#include <hip/hip_bf16.h>
#include <stdint.h>

typedef short bf16x8 __attribute__((ext_vector_type(8)));
typedef short s16x4 __attribute__((ext_vector_type(4)));
typedef float f32x16 __attribute__((ext_vector_type(16)));
typedef float f32x4 __attribute__((ext_vector_type(4)));
typedef float f32x2 __attribute__((ext_vector_type(2)));
typedef unsigned u32x4 __attribute__((ext_vector_type(4)));
typedef unsigned u32x2 __attribute__((ext_vector_type(2)));
typedef unsigned short u16;
typedef __bf16 bf2_t __attribute__((ext_vector_type(2)));
typedef __attribute__((address_space(3))) s16x4* lds_s16x4_p;

constexpr int S = 2048;
constexpr float LOG2E = 1.4426950408889634f;

__device__ __forceinline__ unsigned cvtpk(float lo, float hi) {
    bf2_t v = {(__bf16)lo, (__bf16)hi}; return __builtin_bit_cast(unsigned, v);
}
__device__ __forceinline__ s16x4 trread(const char* p) {
    return __builtin_amdgcn_ds_read_tr16_b64_v4i16((lds_s16x4_p)(p));
}
__device__ __forceinline__ bf16x8 cat8(s16x4 a, s16x4 b) {
    return (bf16x8){a[0], a[1], a[2], a[3], b[0], b[1], b[2], b[3]};
}
__device__ __forceinline__ int swz256(int row) { return ((row & 3) << 2) | ((row >> 2) & 3); }
__device__ __forceinline__ int off_b(int row, int ch) { return 256 * row + 16 * (ch ^ swz256(row)); }
__device__ __forceinline__ void glds16(const void* g, char* l) {
    __builtin_amdgcn_global_load_lds((const unsigned*)g, (unsigned*)l, 16, 0, 0);
}
#define WAITV0() asm volatile("s_waitcnt vmcnt(0)" ::: "memory")
__device__ __forceinline__ float swapmax(float v) {
    auto rr = __builtin_amdgcn_permlane32_swap(__float_as_uint(v), __float_as_uint(v), false, false);
    return fmaxf(__uint_as_float(rr[0]), __uint_as_float(rr[1]));
}
__device__ __forceinline__ float swapsum(float v) {
    auto rr = __builtin_amdgcn_permlane32_swap(__float_as_uint(v), __float_as_uint(v), false, false);
    return __uint_as_float(rr[0]) + __uint_as_float(rr[1]);
}

template <int WM> struct GCfg { static constexpr int ABYTES = WM * 8192, STAGE = WM * 8192 + 16384, NT = 128 * WM, NLD = 4 + 8 / WM; };

template <bool KVA, int WM>
__device__ __forceinline__ void gemm_stage(char* buf, const u16* A, int lda, const u16* W, int ldw, const u16* W2,
                                           int m0, int n0, int k0, int tid) {
    constexpr int NT = GCfg<WM>::NT;
#pragma unroll
    for (int i = 0; i < 4; ++i) {
        int p = tid + NT * i;
        int row = p >> 3, slot = p & 7, ch = slot ^ ((row >> 1) & 7);
        glds16(A + (size_t)(m0 + row) * lda + k0 + ch * 8, buf + p * 16);
    }
#pragma unroll
    for (int i = 0; i < 8 / WM; ++i) {
        int p = tid + NT * i;
        int row = p >> 4, slot = p & 15, ch = slot ^ swz256(row);
        const u16* src;
        if (KVA) {
            src = (ch < 2) ? W + (size_t)(k0 + row) * 16 + ch * 8
                           : ((ch < 4) ? W2 + (size_t)(k0 + row) * 16 + (ch - 2) * 8 : W + (size_t)(k0 + row) * 16);
        } else {
            src = W + (size_t)(k0 + row) * ldw + n0 + ch * 8;
        }
        glds16(src, buf + GCfg<WM>::ABYTES + p * 16);
    }
}

template <bool KVA, int WM, int NS>
__device__ __forceinline__ void gemm_tile(char* lds, const u16* A, int lda, const u16* W, int ldw, const u16* W2,
                                          int m0, int n0, int kbeg, int nkt, f32x16 (&acc)[2][2]) {
    constexpr int STG = GCfg<WM>::STAGE, NLD = GCfg<WM>::NLD;
    const int tid = threadIdx.x, lane = tid & 63, wid = tid >> 6, wr = wid >> 1, wc = wid & 1;
    const int r32 = lane & 31, hi = lane >> 5;
#pragma unroll
    for (int i = 0; i < 2; ++i)
#pragma unroll
        for (int j = 0; j < 2; ++j) acc[i][j] = f32x16{};
#pragma unroll
    for (int i = 0; i < NS - 1; ++i) gemm_stage<KVA, WM>(lds + i * STG, A, lda, W, ldw, W2, m0, n0, kbeg + i * 64, tid);
    const unsigned lbase = (unsigned)(uintptr_t)lds;
    const int asw = ((wr * 64 + r32) >> 1) & 7;
    const int blk = (lane >> 4) & 1, q4 = (lane & 15) >> 2, p4 = lane & 3;
    unsigned xa0[4], wa0[4];
#pragma unroll
    for (int ks = 0; ks < 4; ++ks) xa0[ks] = lbase + 128 * (wr * 64 + r32) + 16 * ((2 * ks + hi) ^ asw);
#pragma unroll
    for (int i = 0; i < 4; ++i)
        wa0[i] = lbase + GCfg<WM>::ABYTES + off_b(8 * hi + 4 * (i & 1) + q4, 4 * (wc * 2 + (i >> 1)) + 2 * blk + (p4 >> 1)) + 8 * (p4 & 1);
#define G_RD(ks, X0, X1, W0, W1, W2, W3) do { \
        asm volatile("ds_read_b128 %0, %6\n\tds_read_b128 %1, %6 offset:4096\n\t" \
                     "ds_read_b64_tr_b16 %2, %7 offset:%11\n\tds_read_b64_tr_b16 %3, %8 offset:%11\n\t" \
                     "ds_read_b64_tr_b16 %4, %9 offset:%11\n\tds_read_b64_tr_b16 %5, %10 offset:%11" \
                     : "=&v"(X0), "=&v"(X1), "=&v"(W0), "=&v"(W1), "=&v"(W2), "=&v"(W3) \
                     : "v"(xa[ks]), "v"(wa[0]), "v"(wa[1]), "v"(wa[2]), "v"(wa[3]), "i"(4096 * (ks)) : "memory"); } while (0)
#define G_WAIT(n, X0, X1, W0, W1, W2, W3) \
        asm volatile("s_waitcnt lgkmcnt(" #n ")" : "+v"(X0), "+v"(X1), "+v"(W0), "+v"(W1), "+v"(W2), "+v"(W3) :: "memory")
#define G_MMA(X0, X1, W0, W1, W2, W3) do { \
        bf16x8 w0_ = cat8(W0, W1), w1_ = cat8(W2, W3); \
        acc[0][0] = __builtin_amdgcn_mfma_f32_32x32x16_bf16(w0_, X0, acc[0][0], 0, 0, 0); \
        acc[0][1] = __builtin_amdgcn_mfma_f32_32x32x16_bf16(w1_, X0, acc[0][1], 0, 0, 0); \
        acc[1][0] = __builtin_amdgcn_mfma_f32_32x32x16_bf16(w0_, X1, acc[1][0], 0, 0, 0); \
        acc[1][1] = __builtin_amdgcn_mfma_f32_32x32x16_bf16(w1_, X1, acc[1][1], 0, 0, 0); } while (0)
    int cb = 0;
    for (int t = 0; t < nkt; ++t) {
        const int rem = nkt - 1 - t;
        if (NS >= 5 && rem >= 3) asm volatile("s_waitcnt vmcnt(%0)" :: "i"(3 * NLD) : "memory");
        else if (NS >= 4 && rem >= 2) asm volatile("s_waitcnt vmcnt(%0)" :: "i"(2 * NLD) : "memory");
        else if (NS >= 3 && rem >= 1) asm volatile("s_waitcnt vmcnt(%0)" :: "i"(NLD) : "memory");
        else WAITV0();
        __builtin_amdgcn_s_barrier();
        asm volatile("" ::: "memory");
        const unsigned bo = cb * STG;
        unsigned xa[4], wa[4];
#pragma unroll
        for (int i = 0; i < 4; ++i) { xa[i] = xa0[i] + bo; wa[i] = wa0[i] + bo; }
        bf16x8 xA0, xA1, xB0, xB1; s16x4 a0, a1, a2, a3, b0, b1, b2, b3;
        G_RD(0, xA0, xA1, a0, a1, a2, a3);
        G_RD(1, xB0, xB1, b0, b1, b2, b3);
        if (t + NS - 1 < nkt) {
            int nb = cb + NS - 1; if (nb >= NS) nb -= NS;
            gemm_stage<KVA, WM>(lds + nb * STG, A, lda, W, ldw, W2, m0, n0, kbeg + (t + NS - 1) * 64, tid);
        }
        G_WAIT(6, xA0, xA1, a0, a1, a2, a3);
        G_MMA(xA0, xA1, a0, a1, a2, a3);
        G_RD(2, xA0, xA1, a0, a1, a2, a3);
        G_WAIT(6, xB0, xB1, b0, b1, b2, b3);
        G_MMA(xB0, xB1, b0, b1, b2, b3);
        G_RD(3, xB0, xB1, b0, b1, b2, b3);
        G_WAIT(6, xA0, xA1, a0, a1, a2, a3);
        G_MMA(xA0, xA1, a0, a1, a2, a3);
        G_WAIT(0, xB0, xB1, b0, b1, b2, b3);
        G_MMA(xB0, xB1, b0, b1, b2, b3);
        if (++cb == NS) cb = 0;
    }
}

__device__ __forceinline__ void store_bf16_tile(u16* dst, int ldd, f32x16 (&acc)[2][2]) {
    const int tid = threadIdx.x, lane = tid & 63, wid = tid >> 6, wr = wid >> 1, wc = wid & 1;
    const int r32 = lane & 31, hi = lane >> 5;
#pragma unroll
    for (int mi = 0; mi < 2; ++mi)
#pragma unroll
        for (int ni = 0; ni < 2; ++ni)
#pragma unroll
            for (int g = 0; g < 4; ++g) {
                u32x2 v = {cvtpk(acc[mi][ni][4 * g], acc[mi][ni][4 * g + 1]), cvtpk(acc[mi][ni][4 * g + 2], acc[mi][ni][4 * g + 3])};
                *(u32x2*)(dst + (size_t)(wr * 64 + mi * 32 + r32) * ldd + wc * 64 + ni * 32 + 8 * g + 4 * hi) = v;
            }
}

constexpr int P_STG = GCfg<4>::STAGE, P_LDS = 3 * P_STG;
struct PTile { const u16* A; const u16* W; const u16* W2; u16* dst; int ldw, ldd, m0, n0, kva; };
__device__ __forceinline__ PTile p_tile(int T, const u16* xa, const u16* wqa, const u16* wka, const u16* wva, const u16* xb,
                                        const u16* ckv, const u16* wq, const u16* wuk, const u16* wuv, u16* QKV, u16* Qa) {
    PTile t;
    if (T < 768) {
        const int pj = T >> 8, nt = (T & 255) >> 3, mt = T & 7;
        t.A = pj == 0 ? xb : ckv; t.W = pj == 0 ? wq : (pj == 1 ? wuk : wuv); t.W2 = t.W; t.ldw = 4096; t.m0 = mt * 256; t.n0 = nt * 128;
        t.dst = QKV + (size_t)pj * (32 * S * 128) + ((size_t)nt * S + mt * 256) * 128; t.ldd = 128; t.kva = 0;
    } else if (T < 800) {
        const int u = T - 768, nt = u >> 3, mt = u & 7;
        t.A = xa; t.W = wqa; t.W2 = wqa; t.ldw = 512; t.m0 = mt * 256; t.n0 = nt * 128;
        t.dst = Qa + (size_t)(mt * 256) * 512 + nt * 128; t.ldd = 512; t.kva = 0;
    } else {
        t.A = xa; t.W = wka; t.W2 = wva; t.ldw = 16; t.m0 = (T - 800) * 256; t.n0 = 0; t.dst = Qa; t.ldd = 512; t.kva = 1;
    }
    return t;
}
__device__ __forceinline__ void p_stage(char* buf, const PTile& t, int k0, int tid) {
#pragma unroll
    for (int i = 0; i < 8; ++i) {
        int p = tid + 256 * i;
        int row = p >> 3, slot = p & 7, ch = slot ^ ((row >> 1) & 7);
        glds16(t.A + (size_t)(t.m0 + row) * 512 + k0 + ch * 8, buf + p * 16);
    }
#pragma unroll
    for (int i = 0; i < 4; ++i) {
        int p = tid + 256 * i;
        int row = p >> 4, slot = p & 15, ch = slot ^ swz256(row);
        const u16* src = t.W + (size_t)(k0 + row) * t.ldw + t.n0 + ch * 8;
        if (t.kva) src = (ch < 2) ? t.W + (size_t)(k0 + row) * 16 + ch * 8
                                  : ((ch < 4) ? t.W2 + (size_t)(k0 + row) * 16 + (ch - 2) * 8 : t.W + (size_t)(k0 + row) * 16);
        glds16(src, buf + 32768 + p * 16);
    }
}

__global__ __launch_bounds__(512, 1) void proj_kernel(const u16* __restrict__ xin, const u16* __restrict__ wqa, const u16* __restrict__ wka,
                                                      const u16* __restrict__ wva, const u16* __restrict__ xb, const u16* __restrict__ ckv,
                                                      const u16* __restrict__ wq, const u16* __restrict__ wuk, const u16* __restrict__ wuv,
                                                      u16* __restrict__ QKV, u16* __restrict__ Qa, u16* __restrict__ Ka, u16* __restrict__ Vimg) {
    extern __shared__ __attribute__((aligned(16))) char lds[];
    const int tid = threadIdx.x, lane = tid & 63, wid = tid >> 6, wr = wid >> 1, wc = wid & 1;
    const int r32 = lane & 31, hi = lane >> 5;
    const int b = blockIdx.x, jb = b >> 3, Tb = (b & 7) * 101 + jb;
    const int ntile = jb < 5 ? 4 : 3, total = ntile * 8;
    PTile cur = p_tile(Tb, xin, wqa, wka, wva, xb, ckv, wq, wuk, wuv, QKV, Qa);
    PTile nxt = p_tile(Tb + 32, xin, wqa, wka, wva, xb, ckv, wq, wuk, wuv, QKV, Qa);
    const bool loader = __builtin_amdgcn_readfirstlane(wid) < 4;
    if (loader) { p_stage(lds, cur, 0, tid); p_stage(lds + P_STG, cur, 64, tid); }
    const unsigned lbase = (unsigned)(uintptr_t)lds;
    const int asw = ((wr * 64 + r32) >> 1) & 7;
    const int blk = (lane >> 4) & 1, q4 = (lane & 15) >> 2, p4 = lane & 3;
    unsigned xa0[4], wa0[4];
#pragma unroll
    for (int ks = 0; ks < 4; ++ks) xa0[ks] = lbase + 128 * (wr * 64 + r32) + 16 * ((2 * ks + hi) ^ asw);
#pragma unroll
    for (int i = 0; i < 4; ++i)
        wa0[i] = lbase + 32768 + off_b(8 * hi + 4 * (i & 1) + q4, 4 * (wc * 2 + (i >> 1)) + 2 * blk + (p4 >> 1)) + 8 * (p4 & 1);
    f32x16 acc[2][2];
    int cb = 0, ti = 0, epi_stores = 0;
    for (int gs = 0; gs < total; ++gs) {
        const int kt = gs & 7;
        if (kt == 0) {
#pragma unroll
            for (int i = 0; i < 2; ++i)
#pragma unroll
                for (int j = 0; j < 2; ++j) acc[i][j] = f32x16{};
        }
        if (loader) {
            if (gs + 1 >= total) WAITV0();
            else asm volatile("s_waitcnt vmcnt(12)" ::: "memory");
        }
        __builtin_amdgcn_s_barrier();
        asm volatile("" ::: "memory");
        const unsigned bo = cb * P_STG;
        unsigned xa[4], wa[4];
#pragma unroll
        for (int i = 0; i < 4; ++i) { xa[i] = xa0[i] + bo; wa[i] = wa0[i] + bo; }
        bf16x8 xA0, xA1, xB0, xB1; s16x4 a0, a1, a2, a3, b0, b1, b2, b3;
        G_RD(0, xA0, xA1, a0, a1, a2, a3);
        G_RD(1, xB0, xB1, b0, b1, b2, b3);
        if (loader && gs + 2 < total) {
            int nb = cb + 2; if (nb >= 3) nb -= 3;
            if (kt < 6) p_stage(lds + nb * P_STG, cur, (kt + 2) * 64, tid);
            else p_stage(lds + nb * P_STG, nxt, (kt - 6) * 64, tid);
        }
        G_WAIT(6, xA0, xA1, a0, a1, a2, a3);
        G_MMA(xA0, xA1, a0, a1, a2, a3);
        G_RD(2, xA0, xA1, a0, a1, a2, a3);
        G_WAIT(6, xB0, xB1, b0, b1, b2, b3);
        G_MMA(xB0, xB1, b0, b1, b2, b3);
        G_RD(3, xB0, xB1, b0, b1, b2, b3);
        G_WAIT(6, xA0, xA1, a0, a1, a2, a3);
        G_MMA(xA0, xA1, a0, a1, a2, a3);
        G_WAIT(0, xB0, xB1, b0, b1, b2, b3);
        G_MMA(xB0, xB1, b0, b1, b2, b3);
        if (kt == 7) {
            if (!cur.kva) {
                __builtin_amdgcn_s_barrier();
                asm volatile("" ::: "memory");
                if (loader) {
                    char* ep = lds + bo + wid * 8192;
#pragma unroll
                    for (int mi = 0; mi < 2; ++mi)
#pragma unroll
                        for (int ni = 0; ni < 2; ++ni)
#pragma unroll
                            for (int g = 0; g < 4; ++g) {
                                u32x2 v = {cvtpk(acc[mi][ni][4 * g], acc[mi][ni][4 * g + 1]), cvtpk(acc[mi][ni][4 * g + 2], acc[mi][ni][4 * g + 3])};
                                *(u32x2*)(ep + mi * 4096 + r32 * 128 + 16 * ((ni * 4 + g) ^ (r32 & 7)) + 8 * hi) = v;
                            }
                    asm volatile("s_waitcnt lgkmcnt(0)" ::: "memory");
                } else {
                    char* ep = lds + bo + 32768 + (wid - 4) * 4096;
#pragma unroll
                    for (int mi = 0; mi < 2; ++mi) {
#pragma unroll
                        for (int ni = 0; ni < 2; ++ni)
#pragma unroll
                            for (int g = 0; g < 4; ++g) {
                                u32x2 v = {cvtpk(acc[mi][ni][4 * g], acc[mi][ni][4 * g + 1]), cvtpk(acc[mi][ni][4 * g + 2], acc[mi][ni][4 * g + 3])};
                                *(u32x2*)(ep + r32 * 128 + 16 * ((ni * 4 + g) ^ (r32 & 7)) + 8 * hi) = v;
                            }
                        asm volatile("s_waitcnt lgkmcnt(0)" ::: "memory");
#pragma unroll
                        for (int i = 0; i < 4; ++i) {
                            const int idx = lane + 64 * i, row = idx >> 3, ch = idx & 7;
                            u32x4 v = *(const u32x4*)(ep + row * 128 + 16 * (ch ^ (row & 7)));
                            *(u32x4*)(cur.dst + (size_t)(wr * 64 + mi * 32 + row) * cur.ldd + wc * 64 + ch * 8) = v;
                        }
                        asm volatile("s_waitcnt lgkmcnt(0)" ::: "memory");
                    }
                }
                __builtin_amdgcn_s_barrier();
                asm volatile("" ::: "memory");
                if (!loader) {
                    const int w2 = wid - 4, wr2 = w2 >> 1;
                    const char* ep = lds + bo + w2 * 8192;
#pragma unroll
                    for (int i = 0; i < 8; ++i) {
                        const int idx = lane + 64 * i, row = idx >> 3, ch = idx & 7;
                        u32x4 v = *(const u32x4*)(ep + row * 128 + 16 * (ch ^ (row & 7)));
                        *(u32x4*)(cur.dst + (size_t)(wr2 * 64 + row) * cur.ldd + wc * 64 + ch * 8) = v;
                    }
                    asm volatile("s_waitcnt lgkmcnt(0)" ::: "memory");
                }
                epi_stores = 1;
            } else {
                epi_stores = 0;
                if (wc == 0) {
#pragma unroll
                    for (int mi = 0; mi < 2; ++mi) {
                        const int m = cur.m0 + wr * 64 + mi * 32 + r32;
#pragma unroll
                        for (int g = 0; g < 2; ++g) {
                            u32x2 v = {cvtpk(acc[mi][0][4 * g], acc[mi][0][4 * g + 1]), cvtpk(acc[mi][0][4 * g + 2], acc[mi][0][4 * g + 3])};
                            *(u32x2*)(Ka + (size_t)m * 16 + 8 * g + 4 * hi) = v;
                        }
                        const int s = m >> 4, kk = m & 15, hh = (kk >> 2) & 1, j = (kk & 3) + 4 * (kk >> 3);
#pragma unroll
                        for (int r = 8; r < 16; ++r) {
                            const int d = (r & 3) + 8 * ((r >> 2) - 2) + 4 * hi;
                            Vimg[((s * 17 + d) * 2 + hh) * 8 + j] = (u16)(cvtpk(acc[mi][0][r], 0.f) & 0xffff);
                        }
                        if (hi == 0) Vimg[((s * 17 + 16) * 2 + hh) * 8 + j] = (u16)0x3F80;
                    }
                }
            }
            ++ti;
            cur = nxt;
            if (ti + 1 < ntile) nxt = p_tile(Tb + 32 * (ti + 1), xin, wqa, wka, wva, xb, ckv, wq, wuk, wuv, QKV, Qa);
        }
        if (++cb == 3) cb = 0;
    }
}

constexpr int O_NS = 5, O_LDS = O_NS * GCfg<2>::STAGE;
__global__ __launch_bounds__(256, 1) void oproj_kernel(const u16* __restrict__ ctxB, const u16* __restrict__ wo_b,
                                                       const u16* __restrict__ ctxA, const u16* __restrict__ wo_a,
                                                       float* __restrict__ part, u16* __restrict__ out) {
    extern __shared__ __attribute__((aligned(16))) char lds[];
    const int b = blockIdx.x;
    f32x16 acc[2][2];
    if (b < 256) {
        const int sp = b & 3, nt = (b >> 2) & 3, mt = b >> 4;
        gemm_tile<false, 2, O_NS>(lds, ctxB, 4096, wo_b, 512, wo_b, mt * 128, nt * 128, sp * 1024, 16, acc);
        const int tid = threadIdx.x, lane = tid & 63, wid = tid >> 6, wr = wid >> 1, wc = wid & 1;
        const int r32 = lane & 31, hi = lane >> 5;
        float* dst = part + (size_t)sp * S * 512;
#pragma unroll
        for (int mi = 0; mi < 2; ++mi)
#pragma unroll
            for (int ni = 0; ni < 2; ++ni)
#pragma unroll
                for (int g = 0; g < 4; ++g) {
                    f32x4 v = {acc[mi][ni][4 * g], acc[mi][ni][4 * g + 1], acc[mi][ni][4 * g + 2], acc[mi][ni][4 * g + 3]};
                    *(f32x4*)(dst + (size_t)(mt * 128 + wr * 64 + mi * 32 + r32) * 512 + nt * 128 + wc * 64 + ni * 32 + 8 * g + 4 * hi) = v;
                }
    } else {
        const int u = b - 256, nt = u & 3, mt = u >> 2;
        gemm_tile<false, 2, O_NS>(lds, ctxA, 512, wo_a, 512, wo_a, mt * 128, nt * 128, 0, 8, acc);
        store_bf16_tile(out + (size_t)(mt * 128) * 1024 + nt * 128, 1024, acc);
    }
}

__global__ __launch_bounds__(256) void reduce_kernel(const float* __restrict__ part, u16* __restrict__ out) {
    const int i = blockIdx.x * 256 + threadIdx.x;
    const f32x4* p = (const f32x4*)part;
    f32x4 a = p[i], b2 = p[i + 262144], c = p[i + 2 * 262144], d = p[i + 3 * 262144];
    f32x4 s = (a + b2) + (c + d);
    const int m = i >> 7, n = (i & 127) * 4;
    u32x2 v = {cvtpk(s[0], s[1]), cvtpk(s[2], s[3])};
    *(u32x2*)(out + (size_t)m * 1024 + 512 + n) = v;
}

constexpr int AB_LDS = 163840;

__device__ __forceinline__ void ab_stage1(char* buf, const u16* P, int t, int tid) {
    const char* base = (const char*)(P + (size_t)t * 128 * 128);
    const unsigned voff = (unsigned)(((tid >> 4) * 128 + ((tid & 15) ^ swz256(tid >> 4)) * 8) * 2);
#pragma unroll
    for (int i = 0; i < 4; ++i) glds16(base + (voff + 8192u * i), buf + (tid + 512 * i) * 16);
}
__device__ __forceinline__ void ab_qk(f32x16& s0, f32x16& s1, const char* kb, const char* ql, int kh, int rg, int r32, int hi) {
    s0 = f32x16{}; s1 = f32x16{};
#pragma unroll
    for (int d0 = 0; d0 < 8; ++d0) {
        bf16x8 qf = *(const bf16x8*)(ql + off_b(32 * rg + r32, 2 * d0 + hi));
        bf16x8 k0 = *(const bf16x8*)(kb + off_b(64 * kh + r32, 2 * d0 + hi));
        bf16x8 k1 = *(const bf16x8*)(kb + off_b(64 * kh + 32 + r32, 2 * d0 + hi));
        s0 = __builtin_amdgcn_mfma_f32_32x32x16_bf16(k0, qf, s0, 0, 0, 0);
        s1 = __builtin_amdgcn_mfma_f32_32x32x16_bf16(k1, qf, s1, 0, 0, 0);
    }
}
__device__ __forceinline__ void ab_mask(f32x16& s0, f32x16& s1, int dq) {
    const float NEG = -__builtin_inff();
#pragma unroll
    for (int r = 0; r < 16; ++r) {
        const int c = (r & 3) + 8 * (r >> 2);
        if (c > dq) s0[r] = NEG;
        if (c + 32 > dq) s1[r] = NEG;
    }
}
__device__ __forceinline__ float ab_max(const f32x16& s0, const f32x16& s1) {
    float pm = s0[0];
#pragma unroll
    for (int r = 1; r < 16; ++r) pm = fmaxf(pm, s0[r]);
#pragma unroll
    for (int r = 0; r < 16; ++r) pm = fmaxf(pm, s1[r]);
    return swapmax(pm);
}
constexpr float AB_C2 = 0.08838834764831845f * LOG2E;
__device__ __forceinline__ void ab_exp(f32x16& s0, f32x16& s1, float m, float& l, bf16x8 (&pf)[4]) {
    const float mc = -m * AB_C2;
    float ps = 0.f;
#pragma unroll
    for (int r = 0; r < 16; ++r) { s0[r] = __builtin_amdgcn_exp2f(fmaf(s0[r], AB_C2, mc)); ps += s0[r]; }
#pragma unroll
    for (int r = 0; r < 16; ++r) { s1[r] = __builtin_amdgcn_exp2f(fmaf(s1[r], AB_C2, mc)); ps += s1[r]; }
    l += ps;
#pragma unroll
    for (int s = 0; s < 2; ++s) {
        u32x4 w0 = {cvtpk(s0[8 * s], s0[8 * s + 1]), cvtpk(s0[8 * s + 2], s0[8 * s + 3]), cvtpk(s0[8 * s + 4], s0[8 * s + 5]), cvtpk(s0[8 * s + 6], s0[8 * s + 7])};
        u32x4 w1 = {cvtpk(s1[8 * s], s1[8 * s + 1]), cvtpk(s1[8 * s + 2], s1[8 * s + 3]), cvtpk(s1[8 * s + 4], s1[8 * s + 5]), cvtpk(s1[8 * s + 6], s1[8 * s + 7])};
        pf[s] = __builtin_bit_cast(bf16x8, w0); pf[2 + s] = __builtin_bit_cast(bf16x8, w1);
    }
}
__device__ __forceinline__ void ab_pv(f32x16 (&o)[4], const unsigned (&va)[8], const bf16x8 (&pf)[4]) {
#define V_RD(ks, h, R) asm volatile( \
        "ds_read_b64_tr_b16 %0, %4 offset:%8\n\tds_read_b64_tr_b16 %1, %5 offset:%8\n\t" \
        "ds_read_b64_tr_b16 %2, %6 offset:%8\n\tds_read_b64_tr_b16 %3, %7 offset:%8" \
        : "=&v"(R[0]), "=&v"(R[1]), "=&v"(R[2]), "=&v"(R[3]) \
        : "v"(va[4 * h]), "v"(va[4 * h + 1]), "v"(va[4 * h + 2]), "v"(va[4 * h + 3]), "i"(4096 * (ks)))
#define V_WAIT(n, R) asm volatile("s_waitcnt lgkmcnt(" #n ")" : "+v"(R[0]), "+v"(R[1]), "+v"(R[2]), "+v"(R[3]))
#define V_MMA(ks, h, R) do { \
        o[2 * h] = __builtin_amdgcn_mfma_f32_32x32x16_bf16(cat8(R[0], R[1]), pf[ks], o[2 * h], 0, 0, 0); \
        o[2 * h + 1] = __builtin_amdgcn_mfma_f32_32x32x16_bf16(cat8(R[2], R[3]), pf[ks], o[2 * h + 1], 0, 0, 0); } while (0)
    s16x4 ra[4], rb[4];
    V_RD(0, 0, ra); V_RD(0, 1, rb);
    V_WAIT(4, ra); V_MMA(0, 0, ra); V_RD(1, 0, ra);
    V_WAIT(4, rb); V_MMA(0, 1, rb); V_RD(1, 1, rb);
    V_WAIT(4, ra); V_MMA(1, 0, ra); V_RD(2, 0, ra);
    V_WAIT(4, rb); V_MMA(1, 1, rb); V_RD(2, 1, rb);
    V_WAIT(4, ra); V_MMA(2, 0, ra); V_RD(3, 0, ra);
    V_WAIT(4, rb); V_MMA(2, 1, rb); V_RD(3, 1, rb);
    V_WAIT(4, ra); V_MMA(3, 0, ra);
    V_WAIT(0, rb); V_MMA(3, 1, rb);
}
__device__ __forceinline__ void ab_rescale(float pm, float& m, float& l, f32x16 (&o)[4]) {
    if (!__all(pm <= m)) {
        const float mn = fmaxf(m, pm);
        const float alpha = __builtin_amdgcn_exp2f((m - mn) * AB_C2);
        m = mn; l *= alpha;
#pragma unroll
        for (int i = 0; i < 4; ++i)
#pragma unroll
            for (int r = 0; r < 16; ++r) o[i][r] *= alpha;
    }
}

__global__ __launch_bounds__(512, 1) void attnB_kernel(const u16* __restrict__ QKV, u16* __restrict__ ctx) {
    extern __shared__ __attribute__((aligned(16))) char lds[];
    const int tid = threadIdx.x, lane = tid & 63, wid = __builtin_amdgcn_readfirstlane(tid >> 6);
    const int rg = wid & 3, kh = wid >> 2, r32 = lane & 31, hi = lane >> 5;
    const int b = blockIdx.x, jj = b >> 3, head = (b & 7) * 4 + (jj >> 3), x = jj & 7;
    const u16* Qh = QKV + (size_t)head * S * 128;
    const u16* Kh = QKV + (size_t)32 * S * 128 + (size_t)head * S * 128;
    const u16* Vh = QKV + (size_t)64 * S * 128 + (size_t)head * S * 128;
    const int blk = (lane >> 4) & 1, q4 = (lane & 15) >> 2, p4 = lane & 3;
    char* Kl = lds; char* Vl = lds + 65536; char* Ql = lds + 131072;
    unsigned va0[8];
#pragma unroll
    for (int i = 0; i < 8; ++i)
        va0[i] = (unsigned)(uintptr_t)Vl + off_b(64 * kh + 4 * hi + 8 * (i & 1) + q4, 4 * (i >> 1) + 2 * blk + (p4 >> 1)) + 8 * (p4 & 1);
    const int dq = 32 * rg + r32 - 64 * kh - 4 * hi;
    for (int pass = 0; pass < 2; ++pass) {
        const int qb = pass == 0 ? 15 - x : x;
        const int nt = qb + 1;
        int tz = tid; asm volatile("" : "+v"(tz));
        ab_stage1(Kl, Kh, 0, tz);
        ab_stage1(Vl, Vh, 0, tz);
        if (nt > 1) ab_stage1(Kl + 32768, Kh, 1, tz);
        ab_stage1(Ql, Qh, qb, tz);
        f32x16 o[4];
#pragma unroll
        for (int i = 0; i < 4; ++i) o[i] = f32x16{};
        float l = 0.f;
        WAITV0();
        __syncthreads();
        f32x16 s0, s1;
        ab_qk(s0, s1, Kl, Ql, kh, rg, r32, hi);
        if (nt == 1) ab_mask(s0, s1, dq);
        float m = fmaxf(ab_max(s0, s1), -1e30f);
        for (int t = 0; t < nt - 1; ++t) {
            WAITV0();
            __syncthreads();
            if (t + 2 < nt) ab_stage1(Kl + (t & 1) * 32768, Kh, t + 2, tid);
            ab_stage1(Vl + ((t + 1) & 1) * 32768, Vh, t + 1, tid);
            f32x16 n0, n1;
            bf16x8 pf[4];
            if (kh == 0) {
                ab_qk(n0, n1, Kl + ((t + 1) & 1) * 32768, Ql, kh, rg, r32, hi);
                __builtin_amdgcn_sched_barrier(0);
                ab_exp(s0, s1, m, l, pf);
                __builtin_amdgcn_sched_barrier(0);
                ab_pv(o, va0, pf);
            } else {
                ab_exp(s0, s1, m, l, pf);
                __builtin_amdgcn_sched_barrier(0);
                ab_pv(o, va0, pf);
                __builtin_amdgcn_sched_barrier(0);
                ab_qk(n0, n1, Kl + ((t + 1) & 1) * 32768, Ql, kh, rg, r32, hi);
            }
            __builtin_amdgcn_sched_barrier(0);
#pragma unroll
            for (int i = 0; i < 8; ++i) va0[i] ^= 32768u;
            if (t + 2 == nt) ab_mask(n0, n1, dq);
            const float pm = ab_max(n0, n1);
            ab_rescale(pm, m, l, o);
            s0 = n0; s1 = n1;
        }
        {
            WAITV0();
            __syncthreads();
            bf16x8 pf[4];
            ab_exp(s0, s1, m, l, pf);
            ab_pv(o, va0, pf);
            if (nt & 1) { } else {
#pragma unroll
                for (int i = 0; i < 8; ++i) va0[i] ^= 32768u; }
        }
        l = swapsum(l);
        __syncthreads();
        float* mo = (float*)lds;
        float* ml = (float*)(lds + 65536);
        if (kh == 1) {
#pragma unroll
            for (int i = 0; i < 4; ++i)
#pragma unroll
                for (int r = 0; r < 16; ++r) mo[(rg * 64 + i * 16 + r) * 64 + lane] = o[i][r];
            ml[(rg * 2) * 64 + lane] = m; ml[(rg * 2 + 1) * 64 + lane] = l;
        }
        __syncthreads();
        if (kh == 0) {
            const float m2 = ml[(rg * 2) * 64 + lane], l2 = ml[(rg * 2 + 1) * 64 + lane];
            const float mn = fmaxf(m, m2);
            const float a1 = __builtin_amdgcn_exp2f((m - mn) * AB_C2), a2 = __builtin_amdgcn_exp2f((m2 - mn) * AB_C2);
            const float inv = 1.f / (l * a1 + l2 * a2);
            const float f1 = a1 * inv, f2 = a2 * inv;
            u16* dst = ctx + (size_t)(128 * qb + 32 * rg + r32) * 4096 + head * 128;
#pragma unroll
            for (int i = 0; i < 4; ++i)
#pragma unroll
                for (int g = 0; g < 4; ++g) {
                    float v[4];
#pragma unroll
                    for (int e = 0; e < 4; ++e) v[e] = o[i][4 * g + e] * f1 + mo[(rg * 64 + i * 16 + 4 * g + e) * 64 + lane] * f2;
                    u32x2 w = {cvtpk(v[0], v[1]), cvtpk(v[2], v[3])};
                    *(u32x2*)(dst + 32 * i + 8 * g + 4 * hi) = w;
                }
        }
        __syncthreads();
    }
}

constexpr int AA_LDS = 65536 + 69632 + 8 * 64 * 10 * 4;
__global__ __launch_bounds__(1024, 1) void attnA_kernel(const u16* __restrict__ Qa, const u16* __restrict__ Ka,
                                                        const u16* __restrict__ Vimg, u16* __restrict__ ctxA) {
    extern __shared__ __attribute__((aligned(16))) char lds[];
    const int tid = threadIdx.x, lane = tid & 63, wid = __builtin_amdgcn_readfirstlane(tid >> 6);
    const int r32 = lane & 31, hi = lane >> 5, tk = wid & 7, kh = wid >> 3;
#pragma unroll
    for (int i = 0; i < 4; ++i) { int p = tid + 1024 * i; glds16(Ka + p * 8, lds + p * 16); }
#pragma unroll
    for (int i = 0; i < 5; ++i) { int p = tid + 1024 * i; if (p < 4352) glds16(Vimg + p * 8, lds + 65536 + p * 16); }
    const int g = blockIdx.x * 8 + tk, head = g & 31, qblk = g >> 5;
    const int row = 32 * qblk + r32;
    const bf16x8 qf = *(const bf16x8*)(Qa + (size_t)row * 512 + head * 16 + 8 * hi);
    constexpr float C2 = 0.25f * LOG2E;
    const int dd = r32 <= 16 ? r32 : (r32 & 15);
    const char* kp = lds + kh * 32768 + r32 * 32 + 16 * hi;
    const char* vp = lds + 65536 + kh * (64 * 544) + (dd * 2 + hi) * 16;
    f32x16 o = f32x16{};
    float m = -1e30f;
    WAITV0();
    __syncthreads();
    for (int it = 0; it < 16; ++it) {
        bf16x8 k0 = *(const bf16x8*)(kp + it * 2048);
        bf16x8 k1 = *(const bf16x8*)(kp + it * 2048 + 1024);
        f32x16 s0 = __builtin_amdgcn_mfma_f32_32x32x16_bf16(k0, qf, f32x16{}, 0, 0, 0);
        f32x16 s1 = __builtin_amdgcn_mfma_f32_32x32x16_bf16(k1, qf, f32x16{}, 0, 0, 0);
        float pm = s0[0];
#pragma unroll
        for (int r = 1; r < 16; ++r) pm = fmaxf(pm, s0[r]);
#pragma unroll
        for (int r = 0; r < 16; ++r) pm = fmaxf(pm, s1[r]);
        pm = swapmax(pm);
        if (!__all(pm <= m)) {
            const float mn = fmaxf(m, pm);
            const float alpha = __builtin_amdgcn_exp2f((m - mn) * C2);
            m = mn;
#pragma unroll
            for (int r = 0; r < 9; ++r) o[r] *= alpha;
        }
        const float mc = -m * C2;
        {
            const f32x2 c2v = {C2, C2}, mcv = {mc, mc};
#pragma unroll
            for (int r = 0; r < 16; r += 2) {
                f32x2 a = {s0[r], s0[r + 1]}, b = {s1[r], s1[r + 1]};
                a = __builtin_elementwise_fma(a, c2v, mcv); b = __builtin_elementwise_fma(b, c2v, mcv);
                s0[r] = __builtin_amdgcn_exp2f(a[0]); s0[r + 1] = __builtin_amdgcn_exp2f(a[1]);
                s1[r] = __builtin_amdgcn_exp2f(b[0]); s1[r + 1] = __builtin_amdgcn_exp2f(b[1]);
            }
        }
#pragma unroll
        for (int s = 0; s < 2; ++s) {
            u32x4 w0 = {cvtpk(s0[8 * s], s0[8 * s + 1]), cvtpk(s0[8 * s + 2], s0[8 * s + 3]), cvtpk(s0[8 * s + 4], s0[8 * s + 5]), cvtpk(s0[8 * s + 6], s0[8 * s + 7])};
            u32x4 w1 = {cvtpk(s1[8 * s], s1[8 * s + 1]), cvtpk(s1[8 * s + 2], s1[8 * s + 3]), cvtpk(s1[8 * s + 4], s1[8 * s + 5]), cvtpk(s1[8 * s + 6], s1[8 * s + 7])};
            bf16x8 v0 = *(const bf16x8*)(vp + (4 * it + s) * 544);
            bf16x8 v1 = *(const bf16x8*)(vp + (4 * it + 2 + s) * 544);
            o = __builtin_amdgcn_mfma_f32_32x32x16_bf16(v0, __builtin_bit_cast(bf16x8, w0), o, 0, 0, 0);
            o = __builtin_amdgcn_mfma_f32_32x32x16_bf16(v1, __builtin_bit_cast(bf16x8, w1), o, 0, 0, 0);
        }
    }
    float* mg = (float*)(lds + 65536 + 69632) + tk * 640;
    if (kh == 1) {
#pragma unroll
        for (int r = 0; r < 9; ++r) mg[r * 64 + lane] = o[r];
        mg[9 * 64 + lane] = m;
    }
    __syncthreads();
    if (kh == 0) {
        const float m2 = mg[9 * 64 + lane];
        const float mn = fmaxf(m, m2);
        const float a1 = __builtin_amdgcn_exp2f((m - mn) * C2), a2 = __builtin_amdgcn_exp2f((m2 - mn) * C2);
        float ov[9];
#pragma unroll
        for (int r = 0; r < 9; ++r) ov[r] = o[r] * a1 + mg[r * 64 + lane] * a2;
        const float l = __shfl(ov[8], r32);
        const float inv = 1.f / l;
        u16* dst = ctxA + (size_t)row * 512 + head * 16 + 4 * hi;
#pragma unroll
        for (int g2 = 0; g2 < 2; ++g2) {
            u32x2 w = {cvtpk(ov[4 * g2] * inv, ov[4 * g2 + 1] * inv), cvtpk(ov[4 * g2 + 2] * inv, ov[4 * g2 + 3] * inv)};
            *(u32x2*)(dst + 8 * g2) = w;
        }
    }
}

extern "C" void kernel_launch(void* const* d_in, const int* in_sizes, int n_in,
                              void* d_out, int out_size, void* d_ws, size_t ws_size,
                              hipStream_t stream) {
    const u16* A_x = (const u16*)d_in[0];
    const u16* A_wq = (const u16*)d_in[1];
    const u16* A_wk = (const u16*)d_in[2];
    const u16* A_wv = (const u16*)d_in[3];
    const u16* A_wo = (const u16*)d_in[4];
    const u16* B_x = (const u16*)d_in[5];
    const u16* B_ckv = (const u16*)d_in[6];
    const u16* B_wq = (const u16*)d_in[7];
    const u16* B_wuk = (const u16*)d_in[8];
    const u16* B_wuv = (const u16*)d_in[9];
    const u16* B_wo = (const u16*)d_in[10];
    char* ws = (char*)d_ws;
    const size_t MB = 1 << 20;
    u16* QKV = (u16*)ws;
    u16* ctxB = (u16*)(ws + 48 * MB);
    u16* Qa = (u16*)(ws + 64 * MB);
    u16* ctxA = (u16*)(ws + 66 * MB);
    u16* Ka = (u16*)(ws + 68 * MB);
    u16* Vimg = (u16*)(ws + 68 * MB + 65536);
    float* part = (float*)(ws + 72 * MB);
    u16* out = (u16*)d_out;

    hipFuncSetAttribute((const void*)proj_kernel, hipFuncAttributeMaxDynamicSharedMemorySize, P_LDS);
    hipFuncSetAttribute((const void*)oproj_kernel, hipFuncAttributeMaxDynamicSharedMemorySize, O_LDS);
    hipFuncSetAttribute((const void*)attnB_kernel, hipFuncAttributeMaxDynamicSharedMemorySize, AB_LDS);
    hipFuncSetAttribute((const void*)attnA_kernel, hipFuncAttributeMaxDynamicSharedMemorySize, AA_LDS);

    proj_kernel<<<256, 512, P_LDS, stream>>>(A_x, A_wq, A_wk, A_wv, B_x, B_ckv, B_wq, B_wuk, B_wuv, QKV, Qa, Ka, Vimg);
    attnB_kernel<<<256, 512, AB_LDS, stream>>>(QKV, ctxB);
    attnA_kernel<<<256, 1024, AA_LDS, stream>>>(Qa, Ka, Vimg, ctxA);
    oproj_kernel<<<320, 256, O_LDS, stream>>>(ctxB, B_wo, ctxA, A_wo, part, out);
    reduce_kernel<<<1024, 256, 0, stream>>>(part, out);
}
```

```cpp
#include <hip/hip_runtime.h>
#include <hip/hip_bf16.h>
#include <stdint.h>

typedef short bf16x8 __attribute__((ext_vector_type(8)));
typedef short s16x4 __attribute__((ext_vector_type(4)));
typedef float f32x16 __attribute__((ext_vector_type(16)));
typedef float f32x4 __attribute__((ext_vector_type(4)));
typedef float f32x2 __attribute__((ext_vector_type(2)));
typedef unsigned u32x4 __attribute__((ext_vector_type(4)));
typedef unsigned u32x2 __attribute__((ext_vector_type(2)));
typedef unsigned short u16;
typedef __bf16 bf2_t __attribute__((ext_vector_type(2)));
typedef __attribute__((address_space(3))) s16x4* lds_s16x4_p;

constexpr int S = 2048;
constexpr float LOG2E = 1.4426950408889634f;

__device__ __forceinline__ unsigned cvtpk(float lo, float hi) {
    bf2_t v = {(__bf16)lo, (__bf16)hi}; return __builtin_bit_cast(unsigned, v);
}
__device__ __forceinline__ s16x4 trread(const char* p) {
    return __builtin_amdgcn_ds_read_tr16_b64_v4i16((lds_s16x4_p)(p));
}
__device__ __forceinline__ bf16x8 cat8(s16x4 a, s16x4 b) {
    return (bf16x8){a[0], a[1], a[2], a[3], b[0], b[1], b[2], b[3]};
}
__device__ __forceinline__ int swz256(int row) { return ((row & 3) << 2) | ((row >> 2) & 3); }
__device__ __forceinline__ int off_b(int row, int ch) { return 256 * row + 16 * (ch ^ swz256(row)); }
__device__ __forceinline__ void glds16(const void* g, char* l) {
    __builtin_amdgcn_global_load_lds((const unsigned*)g, (unsigned*)l, 16, 0, 0);
}
#define WAITV0() asm volatile("s_waitcnt vmcnt(0)" ::: "memory")
__device__ __forceinline__ float swapmax(float v) {
    auto rr = __builtin_amdgcn_permlane32_swap(__float_as_uint(v), __float_as_uint(v), false, false);
    return fmaxf(__uint_as_float(rr[0]), __uint_as_float(rr[1]));
}
__device__ __forceinline__ float swapsum(float v) {
    auto rr = __builtin_amdgcn_permlane32_swap(__float_as_uint(v), __float_as_uint(v), false, false);
    return __uint_as_float(rr[0]) + __uint_as_float(rr[1]);
}

template <int WM> struct GCfg { static constexpr int ABYTES = WM * 8192, STAGE = WM * 8192 + 16384, NT = 128 * WM, NLD = 4 + 8 / WM; };

template <bool KVA, int WM>
__device__ __forceinline__ void gemm_stage(char* buf, const u16* A, int lda, const u16* W, int ldw, const u16* W2,
                                           int m0, int n0, int k0, int tid) {
    constexpr int NT = GCfg<WM>::NT;
#pragma unroll
    for (int i = 0; i < 4; ++i) {
        int p = tid + NT * i;
        int row = p >> 3, slot = p & 7, ch = slot ^ ((row >> 1) & 7);
        glds16(A + (size_t)(m0 + row) * lda + k0 + ch * 8, buf + p * 16);
    }
#pragma unroll
    for (int i = 0; i < 8 / WM; ++i) {
        int p = tid + NT * i;
        int row = p >> 4, slot = p & 15, ch = slot ^ swz256(row);
        const u16* src;
        if (KVA) {
            src = (ch < 2) ? W + (size_t)(k0 + row) * 16 + ch * 8
                           : ((ch < 4) ? W2 + (size_t)(k0 + row) * 16 + (ch - 2) * 8 : W + (size_t)(k0 + row) * 16);
        } else {
            src = W + (size_t)(k0 + row) * ldw + n0 + ch * 8;
        }
        glds16(src, buf + GCfg<WM>::ABYTES + p * 16);
    }
}

template <bool KVA, int WM, int NS>
__device__ __forceinline__ void gemm_tile(char* lds, const u16* A, int lda, const u16* W, int ldw, const u16* W2,
                                          int m0, int n0, int kbeg, int nkt, f32x16 (&acc)[2][2]) {
    constexpr int STG = GCfg<WM>::STAGE, NLD = GCfg<WM>::NLD;
    const int tid = threadIdx.x, lane = tid & 63, wid = tid >> 6, wr = wid >> 1, wc = wid & 1;
    const int r32 = lane & 31, hi = lane >> 5;
#pragma unroll
    for (int i = 0; i < 2; ++i)
#pragma unroll
        for (int j = 0; j < 2; ++j) acc[i][j] = f32x16{};
#pragma unroll
    for (int i = 0; i < NS - 1; ++i) gemm_stage<KVA, WM>(lds + i * STG, A, lda, W, ldw, W2, m0, n0, kbeg + i * 64, tid);
    const unsigned lbase = (unsigned)(uintptr_t)lds;
    const int asw = ((wr * 64 + r32) >> 1) & 7;
    const int blk = (lane >> 4) & 1, q4 = (lane & 15) >> 2, p4 = lane & 3;
    unsigned xa0[4], wa0[4];
#pragma unroll
    for (int ks = 0; ks < 4; ++ks) xa0[ks] = lbase + 128 * (wr * 64 + r32) + 16 * ((2 * ks + hi) ^ asw);
#pragma unroll
    for (int i = 0; i < 4; ++i)
        wa0[i] = lbase + GCfg<WM>::ABYTES + off_b(8 * hi + 4 * (i & 1) + q4, 4 * (wc * 2 + (i >> 1)) + 2 * blk + (p4 >> 1)) + 8 * (p4 & 1);
#define G_RD(ks, X0, X1, W0, W1, W2, W3) do { \
        asm volatile("ds_read_b128 %0, %6\n\tds_read_b128 %1, %6 offset:4096\n\t" \
                     "ds_read_b64_tr_b16 %2, %7 offset:%11\n\tds_read_b64_tr_b16 %3, %8 offset:%11\n\t" \
                     "ds_read_b64_tr_b16 %4, %9 offset:%11\n\tds_read_b64_tr_b16 %5, %10 offset:%11" \
                     : "=&v"(X0), "=&v"(X1), "=&v"(W0), "=&v"(W1), "=&v"(W2), "=&v"(W3) \
                     : "v"(xa[ks]), "v"(wa[0]), "v"(wa[1]), "v"(wa[2]), "v"(wa[3]), "i"(4096 * (ks)) : "memory"); } while (0)
#define G_WAIT(n, X0, X1, W0, W1, W2, W3) \
        asm volatile("s_waitcnt lgkmcnt(" #n ")" : "+v"(X0), "+v"(X1), "+v"(W0), "+v"(W1), "+v"(W2), "+v"(W3) :: "memory")
#define G_MMA(X0, X1, W0, W1, W2, W3) do { \
        bf16x8 w0_ = cat8(W0, W1), w1_ = cat8(W2, W3); \
        acc[0][0] = __builtin_amdgcn_mfma_f32_32x32x16_bf16(w0_, X0, acc[0][0], 0, 0, 0); \
        acc[0][1] = __builtin_amdgcn_mfma_f32_32x32x16_bf16(w1_, X0, acc[0][1], 0, 0, 0); \
        acc[1][0] = __builtin_amdgcn_mfma_f32_32x32x16_bf16(w0_, X1, acc[1][0], 0, 0, 0); \
        acc[1][1] = __builtin_amdgcn_mfma_f32_32x32x16_bf16(w1_, X1, acc[1][1], 0, 0, 0); } while (0)
    int cb = 0;
    for (int t = 0; t < nkt; ++t) {
        const int rem = nkt - 1 - t;
        if (NS >= 5 && rem >= 3) asm volatile("s_waitcnt vmcnt(%0)" :: "i"(3 * NLD) : "memory");
        else if (NS >= 4 && rem >= 2) asm volatile("s_waitcnt vmcnt(%0)" :: "i"(2 * NLD) : "memory");
        else if (NS >= 3 && rem >= 1) asm volatile("s_waitcnt vmcnt(%0)" :: "i"(NLD) : "memory");
        else WAITV0();
        __builtin_amdgcn_s_barrier();
        asm volatile("" ::: "memory");
        const unsigned bo = cb * STG;
        unsigned xa[4], wa[4];
#pragma unroll
        for (int i = 0; i < 4; ++i) { xa[i] = xa0[i] + bo; wa[i] = wa0[i] + bo; }
        bf16x8 xA0, xA1, xB0, xB1; s16x4 a0, a1, a2, a3, b0, b1, b2, b3;
        G_RD(0, xA0, xA1, a0, a1, a2, a3);
        G_RD(1, xB0, xB1, b0, b1, b2, b3);
        if (t + NS - 1 < nkt) {
            int nb = cb + NS - 1; if (nb >= NS) nb -= NS;
            gemm_stage<KVA, WM>(lds + nb * STG, A, lda, W, ldw, W2, m0, n0, kbeg + (t + NS - 1) * 64, tid);
        }
        G_WAIT(6, xA0, xA1, a0, a1, a2, a3);
        G_MMA(xA0, xA1, a0, a1, a2, a3);
        G_RD(2, xA0, xA1, a0, a1, a2, a3);
        G_WAIT(6, xB0, xB1, b0, b1, b2, b3);
        G_MMA(xB0, xB1, b0, b1, b2, b3);
        G_RD(3, xB0, xB1, b0, b1, b2, b3);
        G_WAIT(6, xA0, xA1, a0, a1, a2, a3);
        G_MMA(xA0, xA1, a0, a1, a2, a3);
        G_WAIT(0, xB0, xB1, b0, b1, b2, b3);
        G_MMA(xB0, xB1, b0, b1, b2, b3);
        if (++cb == NS) cb = 0;
    }
}

__device__ __forceinline__ void store_bf16_tile(u16* dst, int ldd, f32x16 (&acc)[2][2]) {
    const int tid = threadIdx.x, lane = tid & 63, wid = tid >> 6, wr = wid >> 1, wc = wid & 1;
    const int r32 = lane & 31, hi = lane >> 5;
#pragma unroll
    for (int mi = 0; mi < 2; ++mi)
#pragma unroll
        for (int ni = 0; ni < 2; ++ni)
#pragma unroll
            for (int g = 0; g < 4; ++g) {
                u32x2 v = {cvtpk(acc[mi][ni][4 * g], acc[mi][ni][4 * g + 1]), cvtpk(acc[mi][ni][4 * g + 2], acc[mi][ni][4 * g + 3])};
                *(u32x2*)(dst + (size_t)(wr * 64 + mi * 32 + r32) * ldd + wc * 64 + ni * 32 + 8 * g + 4 * hi) = v;
            }
}

constexpr int P_STG = 65536, P_LDS = 2 * P_STG + 32768;
struct PTile { const u16* A; const u16* W; const u16* W2; u16* dst; int ldw, ldd, m0, n0, kind; };
__device__ __forceinline__ PTile p_tile(int T, const u16* xin, const u16* wqa, const u16* wka, const u16* wva, const u16* xb,
                                        const u16* ckv, const u16* wq, const u16* wuk, const u16* wuv, u16* QKV, u16* Qa) {
    PTile t;
    if (T < 384) {
        const int pj = T >> 7, nt = (T & 127) >> 3, mt = T & 7;
        t.A = pj == 0 ? xb : ckv; t.W = pj == 0 ? wq : (pj == 1 ? wuk : wuv); t.W2 = t.W; t.ldw = 4096; t.m0 = mt * 256; t.n0 = nt * 256;
        t.dst = QKV + (size_t)pj * (32 * S * 128) + ((size_t)nt * 2 * S + mt * 256) * 128; t.ldd = 128; t.kind = 0;
    } else if (T < 400) {
        const int u = T - 384, nt = u >> 3, mt = u & 7;
        t.A = xin; t.W = wqa; t.W2 = wqa; t.ldw = 512; t.m0 = mt * 256; t.n0 = nt * 256;
        t.dst = Qa + (size_t)(mt * 256) * 512 + nt * 256; t.ldd = 512; t.kind = 1;
    } else {
        t.A = xin; t.W = wka; t.W2 = wva; t.ldw = 16; t.m0 = (T - 400) * 256; t.n0 = 0; t.dst = Qa; t.ldd = 512; t.kind = 2;
    }
    return t;
}
__device__ __forceinline__ void p_stage(char* buf, const PTile& t, int k0, int tid) {
    {
        const char* base = (const char*)(t.A + (size_t)t.m0 * 512 + k0);
        const int row = tid >> 3;
        const unsigned voff = (unsigned)((row * 512 + ((tid & 7) ^ ((row >> 1) & 7)) * 8) * 2);
#pragma unroll
        for (int i = 0; i < 4; ++i) glds16(base + (voff + 65536u * i), buf + (tid + 512 * i) * 16);
    }
    const int row = tid >> 4, ch = (tid & 15) ^ swz256(row);
    if (t.kind != 2) {
        const char* base = (const char*)(t.W + (size_t)k0 * t.ldw + t.n0);
        const unsigned voff = (unsigned)((row * t.ldw + ch * 8) * 2), step = (unsigned)(64 * t.ldw);
#pragma unroll
        for (int i = 0; i < 4; ++i)
            glds16(base + (voff + (i & 1) * step + (i >> 1) * 256u), buf + 32768 + (i >> 1) * 16384 + (tid + 512 * (i & 1)) * 16);
    } else {
#pragma unroll
        for (int i = 0; i < 4; ++i) {
            const int r2 = row + 32 * (i & 1);
            const u16* src = ((i >> 1) == 0 && ch >= 2 && ch < 4) ? t.W2 + (size_t)(k0 + r2) * 16 + (ch - 2) * 8
                                                                  : t.W + (size_t)(k0 + r2) * 16 + (ch & 1) * 8;
            glds16(src, buf + 32768 + (i >> 1) * 16384 + (tid + 512 * (i & 1)) * 16);
        }
    }
}

__global__ __launch_bounds__(512, 1) void proj_kernel(const u16* __restrict__ xin, const u16* __restrict__ wqa, const u16* __restrict__ wka,
                                                      const u16* __restrict__ wva, const u16* __restrict__ xb, const u16* __restrict__ ckv,
                                                      const u16* __restrict__ wq, const u16* __restrict__ wuk, const u16* __restrict__ wuv,
                                                      u16* __restrict__ QKV, u16* __restrict__ Qa, u16* __restrict__ Ka, u16* __restrict__ Vimg) {
    extern __shared__ __attribute__((aligned(16))) char lds[];
    const int tid = threadIdx.x, lane = tid & 63, wid = __builtin_amdgcn_readfirstlane(tid >> 6), wr = wid >> 2, wc = wid & 3;
    const int r32 = lane & 31, hi = lane >> 5;
    const int b = blockIdx.x, jb = b >> 3, Tb = (b & 7) * 51 + jb;
    const int ntile = jb < 19 ? 2 : 1, total = ntile * 8;
    PTile cur = p_tile(Tb, xin, wqa, wka, wva, xb, ckv, wq, wuk, wuv, QKV, Qa);
    PTile nxt = p_tile(jb < 19 ? Tb + 32 : Tb, xin, wqa, wka, wva, xb, ckv, wq, wuk, wuv, QKV, Qa);
    p_stage(lds, cur, 0, tid);
    const unsigned lbase = (unsigned)(uintptr_t)lds;
    const int asw = (r32 >> 1) & 7;
    const int blk = (lane >> 4) & 1, q4 = (lane & 15) >> 2, p4 = lane & 3;
    unsigned xa[4], wa[4];
#pragma unroll
    for (int ks = 0; ks < 4; ++ks) xa[ks] = lbase + 128 * (wr * 128 + r32) + 16 * ((2 * ks + hi) ^ asw);
#pragma unroll
    for (int i = 0; i < 4; ++i)
        wa[i] = lbase + 32768 + (wc >> 1) * 16384 + off_b(8 * hi + 4 * (i & 1) + q4, 4 * ((wc & 1) * 2 + (i >> 1)) + 2 * blk + (p4 >> 1)) + 8 * (p4 & 1);
#define P_RD(ks, X, Wf) asm volatile( \
        "ds_read_b128 %0, %8\n\tds_read_b128 %1, %8 offset:4096\n\tds_read_b128 %2, %8 offset:8192\n\tds_read_b128 %3, %8 offset:12288\n\t" \
        "ds_read_b64_tr_b16 %4, %9 offset:%13\n\tds_read_b64_tr_b16 %5, %10 offset:%13\n\t" \
        "ds_read_b64_tr_b16 %6, %11 offset:%13\n\tds_read_b64_tr_b16 %7, %12 offset:%13" \
        : "=&v"(X[0]), "=&v"(X[1]), "=&v"(X[2]), "=&v"(X[3]), "=&v"(Wf[0]), "=&v"(Wf[1]), "=&v"(Wf[2]), "=&v"(Wf[3]) \
        : "v"(xa[ks]), "v"(wa[0]), "v"(wa[1]), "v"(wa[2]), "v"(wa[3]), "i"(4096 * (ks)) : "memory")
#define P_WAIT(n, X, Wf) asm volatile("s_waitcnt lgkmcnt(" #n ")" : "+v"(X[0]), "+v"(X[1]), "+v"(X[2]), "+v"(X[3]), "+v"(Wf[0]), "+v"(Wf[1]), "+v"(Wf[2]), "+v"(Wf[3]) :: "memory")
#define P_MMA(X, Wf) do { bf16x8 w0_ = cat8(Wf[0], Wf[1]), w1_ = cat8(Wf[2], Wf[3]); \
        _Pragma("unroll") for (int mi = 0; mi < 4; ++mi) { \
            acc[mi][0] = __builtin_amdgcn_mfma_f32_32x32x16_bf16(w0_, X[mi], acc[mi][0], 0, 0, 0); \
            acc[mi][1] = __builtin_amdgcn_mfma_f32_32x32x16_bf16(w1_, X[mi], acc[mi][1], 0, 0, 0); } } while (0)
    f32x16 acc[4][2];
    for (int gs = 0; gs < total; ++gs) {
        const int kt = gs & 7;
        if (kt == 0) {
#pragma unroll
            for (int i = 0; i < 4; ++i) { acc[i][0] = f32x16{}; acc[i][1] = f32x16{}; }
        }
        if (gs == 8 && cur.kind != 3) asm volatile("s_waitcnt vmcnt(16)" ::: "memory");
        else WAITV0();
        __builtin_amdgcn_s_barrier();
        asm volatile("" ::: "memory");
        bf16x8 xA[4], xB[4]; s16x4 wA[4], wB[4];
        P_RD(0, xA, wA);
        P_RD(1, xB, wB);
        if (gs + 1 < total) {
            char* nb = lds + ((gs + 1) & 1) * P_STG;
            if (kt < 7) p_stage(nb, cur, (kt + 1) * 64, tid); else p_stage(nb, nxt, 0, tid);
        }
        P_WAIT(8, xA, wA); P_MMA(xA, wA);
        P_RD(2, xA, wA);
        P_WAIT(8, xB, wB); P_MMA(xB, wB);
        P_RD(3, xB, wB);
        P_WAIT(8, xA, wA); P_MMA(xA, wA);
        P_WAIT(0, xB, wB); P_MMA(xB, wB);
#pragma unroll
        for (int i = 0; i < 4; ++i) { xa[i] ^= (unsigned)P_STG; wa[i] ^= (unsigned)P_STG; }
        if (kt == 7) {
            int lz = lane; asm volatile("" : "+v"(lz));
            const int r32e = lz & 31, hie = lz >> 5;
            if (cur.kind != 2) {
                char* ep = lds + 2 * P_STG + wid * 4096;
                u16* dw = cur.kind == 0 ? cur.dst + (size_t)(wc >> 1) * (S * 128) + (wc & 1) * 64 : cur.dst + wc * 64;
#pragma unroll
                for (int mi = 0; mi < 4; ++mi) {
#pragma unroll
                    for (int ni = 0; ni < 2; ++ni)
#pragma unroll
                        for (int g = 0; g < 4; ++g) {
                            u32x2 v = {cvtpk(acc[mi][ni][4 * g], acc[mi][ni][4 * g + 1]), cvtpk(acc[mi][ni][4 * g + 2], acc[mi][ni][4 * g + 3])};
                            *(u32x2*)(ep + r32e * 128 + 16 * ((ni * 4 + g) ^ (r32e & 7)) + 8 * hie) = v;
                        }
                    asm volatile("s_waitcnt lgkmcnt(0)" ::: "memory");
#pragma unroll
                    for (int i = 0; i < 4; ++i) {
                        const int idx = lz + 64 * i, row = idx >> 3, ch = idx & 7;
                        u32x4 v = *(const u32x4*)(ep + row * 128 + 16 * (ch ^ (row & 7)));
                        *(u32x4*)(dw + (size_t)(wr * 128 + mi * 32 + row) * cur.ldd + ch * 8) = v;
                    }
                    asm volatile("s_waitcnt lgkmcnt(0)" ::: "memory");
                }
                cur = nxt;
            } else {
                if (wc == 0) {
#pragma unroll
                    for (int mi = 0; mi < 4; ++mi) {
                        const int m = cur.m0 + wr * 128 + mi * 32 + r32e;
#pragma unroll
                        for (int g = 0; g < 2; ++g) {
                            u32x2 v = {cvtpk(acc[mi][0][4 * g], acc[mi][0][4 * g + 1]), cvtpk(acc[mi][0][4 * g + 2], acc[mi][0][4 * g + 3])};
                            *(u32x2*)(Ka + (size_t)m * 16 + 8 * g + 4 * hie) = v;
                        }
                        const int s = m >> 4, kk = m & 15, hh = (kk >> 2) & 1, j = (kk & 3) + 4 * (kk >> 3);
#pragma unroll
                        for (int r = 8; r < 16; ++r) {
                            const int d = (r & 3) + 8 * ((r >> 2) - 2) + 4 * hie;
                            Vimg[((s * 17 + d) * 2 + hh) * 8 + j] = (u16)(cvtpk(acc[mi][0][r], 0.f) & 0xffff);
                        }
                        if (hie == 0) Vimg[((s * 17 + 16) * 2 + hh) * 8 + j] = (u16)0x3F80;
                    }
                }
                cur = nxt; cur.kind = 3 - 0 * cur.kind;
            }
        }
    }
}

constexpr int O_NS = 2, O_STG = 32768, O_LDS = O_NS * O_STG;
__device__ __forceinline__ void o_stage(char* buf, const u16* A, int lda, const u16* W, int m0, int n0, int k0, int tid) {
    {
        const char* base = (const char*)(A + (size_t)m0 * lda + k0);
        const int row = tid >> 3;
        const unsigned voff = (unsigned)((row * lda + ((tid & 7) ^ ((row >> 1) & 7)) * 8) * 2), step = (unsigned)(128 * lda);
#pragma unroll
        for (int i = 0; i < 2; ++i) glds16(base + (voff + step * i), buf + (tid + 512 * i) * 16);
    }
    {
        const char* base = (const char*)(W + (size_t)k0 * 512 + n0);
        const int row = tid >> 4;
        const unsigned voff = (unsigned)((row * 512 + ((tid & 15) ^ swz256(row)) * 8) * 2);
#pragma unroll
        for (int i = 0; i < 2; ++i) glds16(base + (voff + 32768u * i), buf + 16384 + (tid + 512 * i) * 16);
    }
}
__global__ __launch_bounds__(512, 2) void oproj_kernel(const u16* __restrict__ ctxB, const u16* __restrict__ wo_b,
                                                       const u16* __restrict__ ctxA, const u16* __restrict__ wo_a,
                                                       float* __restrict__ part, u16* __restrict__ out) {
    extern __shared__ __attribute__((aligned(16))) char lds[];
    const int b = blockIdx.x;
    const int tid = threadIdx.x, lane = tid & 63, wid = __builtin_amdgcn_readfirstlane(tid >> 6);
    const int wr = (wid >> 1) & 1, wc = wid & 1, kg = wid >> 2;
    const int r32 = lane & 31, hi = lane >> 5;
    const bool isB = b < 256;
    const int sp = b & 3, nt = isB ? (b >> 2) & 3 : (b & 3), mt = isB ? b >> 4 : (b - 256) >> 2;
    const u16* A = isB ? ctxB : ctxA; const u16* W = isB ? wo_b : wo_a;
    const int lda = isB ? 4096 : 512, kbeg = isB ? sp * 1024 : 0, nkt = isB ? 16 : 8;
    const int m0 = mt * 128, n0 = nt * 128;
#pragma unroll
    for (int i = 0; i < O_NS - 1; ++i) o_stage(lds + i * O_STG, A, lda, W, m0, n0, kbeg + i * 64, tid);
    const unsigned lbase = (unsigned)(uintptr_t)lds;
    const int asw = (r32 >> 1) & 7;
    const int blk = (lane >> 4) & 1, q4 = (lane & 15) >> 2, p4 = lane & 3;
    unsigned xa0[2], wa0[4];
#pragma unroll
    for (int k2 = 0; k2 < 2; ++k2) xa0[k2] = lbase + 128 * (wr * 64 + r32) + 16 * ((2 * (2 * kg + k2) + hi) ^ asw);
#pragma unroll
    for (int i = 0; i < 4; ++i)
        wa0[i] = lbase + 16384 + kg * 8192 + off_b(8 * hi + 4 * (i & 1) + q4, 4 * (wc * 2 + (i >> 1)) + 2 * blk + (p4 >> 1)) + 8 * (p4 & 1);
    f32x16 acc[2][2];
#pragma unroll
    for (int i = 0; i < 2; ++i) { acc[i][0] = f32x16{}; acc[i][1] = f32x16{}; }
    int cb = 0;
    for (int t = 0; t < nkt; ++t) {
        const int rem = nkt - 1 - t;
        if (O_NS >= 4 && rem >= 2) asm volatile("s_waitcnt vmcnt(8)" ::: "memory");
        else if (O_NS >= 3 && rem >= 1) asm volatile("s_waitcnt vmcnt(4)" ::: "memory");
        else WAITV0();
        __builtin_amdgcn_s_barrier();
        asm volatile("" ::: "memory");
        const unsigned bo = cb * O_STG;
        unsigned xa[4], wa[4];
        xa[0] = xa0[0] + bo; xa[1] = xa0[1] + bo; xa[2] = xa[3] = 0;
#pragma unroll
        for (int i = 0; i < 4; ++i) wa[i] = wa0[i] + bo;
        bf16x8 xA0, xA1, xB0, xB1; s16x4 a0, a1, a2, a3, b0, b1, b2, b3;
        G_RD(0, xA0, xA1, a0, a1, a2, a3);
        G_RD(1, xB0, xB1, b0, b1, b2, b3);
        if (t + O_NS - 1 < nkt) {
            int nb = cb + O_NS - 1; if (nb >= O_NS) nb -= O_NS;
            o_stage(lds + nb * O_STG, A, lda, W, m0, n0, kbeg + (t + O_NS - 1) * 64, tid);
        }
        G_WAIT(6, xA0, xA1, a0, a1, a2, a3);
        G_MMA(xA0, xA1, a0, a1, a2, a3);
        G_WAIT(0, xB0, xB1, b0, b1, b2, b3);
        G_MMA(xB0, xB1, b0, b1, b2, b3);
        if (++cb == O_NS) cb = 0;
    }
    __syncthreads();
    float* red = (float*)lds;
    if (kg == 1) {
#pragma unroll
        for (int mi = 0; mi < 2; ++mi)
#pragma unroll
            for (int ni = 0; ni < 2; ++ni)
#pragma unroll
                for (int r = 0; r < 16; ++r) red[(((wid & 3) * 4 + mi * 2 + ni) * 16 + r) * 64 + lane] = acc[mi][ni][r];
    }
    __syncthreads();
    if (kg == 0) {
#pragma unroll
        for (int mi = 0; mi < 2; ++mi)
#pragma unroll
            for (int ni = 0; ni < 2; ++ni)
#pragma unroll
                for (int r = 0; r < 16; ++r) acc[mi][ni][r] += red[((wid * 4 + mi * 2 + ni) * 16 + r) * 64 + lane];
        if (isB) {
            float* dst = part + (size_t)sp * S * 512;
#pragma unroll
            for (int mi = 0; mi < 2; ++mi)
#pragma unroll
                for (int ni = 0; ni < 2; ++ni)
#pragma unroll
                    for (int g = 0; g < 4; ++g) {
                        f32x4 v = {acc[mi][ni][4 * g], acc[mi][ni][4 * g + 1], acc[mi][ni][4 * g + 2], acc[mi][ni][4 * g + 3]};
                        *(f32x4*)(dst + (size_t)(m0 + wr * 64 + mi * 32 + r32) * 512 + n0 + wc * 64 + ni * 32 + 8 * g + 4 * hi) = v;
                    }
        } else {
#pragma unroll
            for (int mi = 0; mi < 2; ++mi)
#pragma unroll
                for (int ni = 0; ni < 2; ++ni)
#pragma unroll
                    for (int g = 0; g < 4; ++g) {
                        u32x2 v = {cvtpk(acc[mi][ni][4 * g], acc[mi][ni][4 * g + 1]), cvtpk(acc[mi][ni][4 * g + 2], acc[mi][ni][4 * g + 3])};
                        *(u32x2*)(out + (size_t)(m0 + wr * 64 + mi * 32 + r32) * 1024 + n0 + wc * 64 + ni * 32 + 8 * g + 4 * hi) = v;
                    }
        }
    }
}

__global__ __launch_bounds__(256) void reduce_kernel(const float* __restrict__ part, u16* __restrict__ out) {
    const int i = blockIdx.x * 256 + threadIdx.x;
    const f32x4* p = (const f32x4*)part;
    f32x4 a = p[i], b2 = p[i + 262144], c = p[i + 2 * 262144], d = p[i + 3 * 262144];
    f32x4 s = (a + b2) + (c + d);
    const int m = i >> 7, n = (i & 127) * 4;
    u32x2 v = {cvtpk(s[0], s[1]), cvtpk(s[2], s[3])};
    *(u32x2*)(out + (size_t)m * 1024 + 512 + n) = v;
}

constexpr int AB_LDS = 163840;

__device__ __forceinline__ void ab_stage1(char* buf, const u16* P, int t, int tid) {
    const char* base = (const char*)(P + (size_t)t * 128 * 128);
    const unsigned voff = (unsigned)(((tid >> 4) * 128 + ((tid & 15) ^ swz256(tid >> 4)) * 8) * 2);
#pragma unroll
    for (int i = 0; i < 4; ++i) glds16(base + (voff + 8192u * i), buf + (tid + 512 * i) * 16);
}
__device__ __forceinline__ void ab_qk(f32x16& s0, f32x16& s1, const char* kb, const char* ql, int kh, int rg, int r32, int hi) {
    s0 = f32x16{}; s1 = f32x16{};
#pragma unroll
    for (int d0 = 0; d0 < 8; ++d0) {
        bf16x8 qf = *(const bf16x8*)(ql + off_b(32 * rg + r32, 2 * d0 + hi));
        bf16x8 k0 = *(const bf16x8*)(kb + off_b(64 * kh + r32, 2 * d0 + hi));
        bf16x8 k1 = *(const bf16x8*)(kb + off_b(64 * kh + 32 + r32, 2 * d0 + hi));
        s0 = __builtin_amdgcn_mfma_f32_32x32x16_bf16(k0, qf, s0, 0, 0, 0);
        s1 = __builtin_amdgcn_mfma_f32_32x32x16_bf16(k1, qf, s1, 0, 0, 0);
    }
}
__device__ __forceinline__ void ab_mask(f32x16& s0, f32x16& s1, int dq) {
    const float NEG = -__builtin_inff();
#pragma unroll
    for (int r = 0; r < 16; ++r) {
        const int c = (r & 3) + 8 * (r >> 2);
        if (c > dq) s0[r] = NEG;
        if (c + 32 > dq) s1[r] = NEG;
    }
}
__device__ __forceinline__ float ab_max(const f32x16& s0, const f32x16& s1) {
    float pm = s0[0];
#pragma unroll
    for (int r = 1; r < 16; ++r) pm = fmaxf(pm, s0[r]);
#pragma unroll
    for (int r = 0; r < 16; ++r) pm = fmaxf(pm, s1[r]);
    return swapmax(pm);
}
constexpr float AB_C2 = 0.08838834764831845f * LOG2E;
__device__ __forceinline__ void ab_exp(f32x16& s0, f32x16& s1, float m, float& l, bf16x8 (&pf)[4]) {
    const float mc = -m * AB_C2;
    float ps = 0.f;
#pragma unroll
    for (int r = 0; r < 16; ++r) { s0[r] = __builtin_amdgcn_exp2f(fmaf(s0[r], AB_C2, mc)); ps += s0[r]; }
#pragma unroll
    for (int r = 0; r < 16; ++r) { s1[r] = __builtin_amdgcn_exp2f(fmaf(s1[r], AB_C2, mc)); ps += s1[r]; }
    l += ps;
#pragma unroll
    for (int s = 0; s < 2; ++s) {
        u32x4 w0 = {cvtpk(s0[8 * s], s0[8 * s + 1]), cvtpk(s0[8 * s + 2], s0[8 * s + 3]), cvtpk(s0[8 * s + 4], s0[8 * s + 5]), cvtpk(s0[8 * s + 6], s0[8 * s + 7])};
        u32x4 w1 = {cvtpk(s1[8 * s], s1[8 * s + 1]), cvtpk(s1[8 * s + 2], s1[8 * s + 3]), cvtpk(s1[8 * s + 4], s1[8 * s + 5]), cvtpk(s1[8 * s + 6], s1[8 * s + 7])};
        pf[s] = __builtin_bit_cast(bf16x8, w0); pf[2 + s] = __builtin_bit_cast(bf16x8, w1);
    }
}
__device__ __forceinline__ void ab_pv(f32x16 (&o)[4], const unsigned (&va)[8], const bf16x8 (&pf)[4]) {
#define V_RD(ks, h, R) asm volatile( \
        "ds_read_b64_tr_b16 %0, %4 offset:%8\n\tds_read_b64_tr_b16 %1, %5 offset:%8\n\t" \
        "ds_read_b64_tr_b16 %2, %6 offset:%8\n\tds_read_b64_tr_b16 %3, %7 offset:%8" \
        : "=&v"(R[0]), "=&v"(R[1]), "=&v"(R[2]), "=&v"(R[3]) \
        : "v"(va[4 * h]), "v"(va[4 * h + 1]), "v"(va[4 * h + 2]), "v"(va[4 * h + 3]), "i"(4096 * (ks)))
#define V_WAIT(n, R) asm volatile("s_waitcnt lgkmcnt(" #n ")" : "+v"(R[0]), "+v"(R[1]), "+v"(R[2]), "+v"(R[3]))
#define V_MMA(ks, h, R) do { \
        o[2 * h] = __builtin_amdgcn_mfma_f32_32x32x16_bf16(cat8(R[0], R[1]), pf[ks], o[2 * h], 0, 0, 0); \
        o[2 * h + 1] = __builtin_amdgcn_mfma_f32_32x32x16_bf16(cat8(R[2], R[3]), pf[ks], o[2 * h + 1], 0, 0, 0); } while (0)
    s16x4 ra[4], rb[4];
    V_RD(0, 0, ra); V_RD(0, 1, rb);
    V_WAIT(4, ra); V_MMA(0, 0, ra); V_RD(1, 0, ra);
    V_WAIT(4, rb); V_MMA(0, 1, rb); V_RD(1, 1, rb);
    V_WAIT(4, ra); V_MMA(1, 0, ra); V_RD(2, 0, ra);
    V_WAIT(4, rb); V_MMA(1, 1, rb); V_RD(2, 1, rb);
    V_WAIT(4, ra); V_MMA(2, 0, ra); V_RD(3, 0, ra);
    V_WAIT(4, rb); V_MMA(2, 1, rb); V_RD(3, 1, rb);
    V_WAIT(4, ra); V_MMA(3, 0, ra);
    V_WAIT(0, rb); V_MMA(3, 1, rb);
}
__device__ __forceinline__ void ab_rescale(float pm, float& m, float& l, f32x16 (&o)[4]) {
    if (!__all((pm - m) * AB_C2 <= 8.f)) {
        const float mn = fmaxf(m, pm);
        const float alpha = __builtin_amdgcn_exp2f((m - mn) * AB_C2);
        m = mn; l *= alpha;
#pragma unroll
        for (int i = 0; i < 4; ++i)
#pragma unroll
            for (int r = 0; r < 16; ++r) o[i][r] *= alpha;
    }
}

__global__ __launch_bounds__(512, 1) void attnB_kernel(const u16* __restrict__ QKV, u16* __restrict__ ctx) {
    extern __shared__ __attribute__((aligned(16))) char lds[];
    const int tid = threadIdx.x, lane = tid & 63, wid = __builtin_amdgcn_readfirstlane(tid >> 6);
    const int rg = wid & 3, kh = wid >> 2, r32 = lane & 31, hi = lane >> 5;
    const int b = blockIdx.x, jj = b >> 3, head = (b & 7) * 4 + (jj >> 3), x = jj & 7;
    const u16* Qh = QKV + (size_t)head * S * 128;
    const u16* Kh = QKV + (size_t)32 * S * 128 + (size_t)head * S * 128;
    const u16* Vh = QKV + (size_t)64 * S * 128 + (size_t)head * S * 128;
    const int blk = (lane >> 4) & 1, q4 = (lane & 15) >> 2, p4 = lane & 3;
    char* Kl = lds; char* Vl = lds + 65536; char* Ql = lds + 131072;
    unsigned va0[8];
#pragma unroll
    for (int i = 0; i < 8; ++i)
        va0[i] = (unsigned)(uintptr_t)Vl + off_b(64 * kh + 4 * hi + 8 * (i & 1) + q4, 4 * (i >> 1) + 2 * blk + (p4 >> 1)) + 8 * (p4 & 1);
    const int dq = 32 * rg + r32 - 64 * kh - 4 * hi;
    for (int pass = 0; pass < 2; ++pass) {
        const int qb = pass == 0 ? 15 - x : x;
        const int nt = qb + 1;
        int tz = tid; asm volatile("" : "+v"(tz));
        ab_stage1(Kl, Kh, 0, tz);
        ab_stage1(Vl, Vh, 0, tz);
        if (nt > 1) ab_stage1(Kl + 32768, Kh, 1, tz);
        ab_stage1(Ql, Qh, qb, tz);
        f32x16 o[4];
#pragma unroll
        for (int i = 0; i < 4; ++i) o[i] = f32x16{};
        float l = 0.f;
        WAITV0();
        __syncthreads();
        f32x16 s0, s1;
        ab_qk(s0, s1, Kl, Ql, kh, rg, r32, hi);
        if (nt == 1) ab_mask(s0, s1, dq);
        float m = fmaxf(ab_max(s0, s1), -1e30f);
        for (int t = 0; t < nt - 1; ++t) {
            WAITV0();
            __syncthreads();
            if (t + 2 < nt) ab_stage1(Kl + (t & 1) * 32768, Kh, t + 2, tid);
            ab_stage1(Vl + ((t + 1) & 1) * 32768, Vh, t + 1, tid);
            f32x16 n0, n1;
            bf16x8 pf[4];
            if (kh == 0) {
                ab_qk(n0, n1, Kl + ((t + 1) & 1) * 32768, Ql, kh, rg, r32, hi);
                __builtin_amdgcn_sched_barrier(0);
                ab_exp(s0, s1, m, l, pf);
                __builtin_amdgcn_sched_barrier(0);
                ab_pv(o, va0, pf);
            } else {
                ab_exp(s0, s1, m, l, pf);
                __builtin_amdgcn_sched_barrier(0);
                ab_pv(o, va0, pf);
                __builtin_amdgcn_sched_barrier(0);
                ab_qk(n0, n1, Kl + ((t + 1) & 1) * 32768, Ql, kh, rg, r32, hi);
            }
            __builtin_amdgcn_sched_barrier(0);
#pragma unroll
            for (int i = 0; i < 8; ++i) va0[i] ^= 32768u;
            if (t + 2 == nt) ab_mask(n0, n1, dq);
            const float pm = ab_max(n0, n1);
            ab_rescale(pm, m, l, o);
            s0 = n0; s1 = n1;
        }
        {
            WAITV0();
            __syncthreads();
            bf16x8 pf[4];
            ab_exp(s0, s1, m, l, pf);
            ab_pv(o, va0, pf);
            if (nt & 1) { } else {
#pragma unroll
                for (int i = 0; i < 8; ++i) va0[i] ^= 32768u; }
        }
        l = swapsum(l);
        __syncthreads();
        float* mo = (float*)lds;
        float* ml = (float*)(lds + 65536);
        if (kh == 1) {
#pragma unroll
            for (int i = 0; i < 4; ++i)
#pragma unroll
                for (int r = 0; r < 16; ++r) mo[(rg * 64 + i * 16 + r) * 64 + lane] = o[i][r];
            ml[(rg * 2) * 64 + lane] = m; ml[(rg * 2 + 1) * 64 + lane] = l;
        }
        __syncthreads();
        if (kh == 0) {
            const float m2 = ml[(rg * 2) * 64 + lane], l2 = ml[(rg * 2 + 1) * 64 + lane];
            const float mn = fmaxf(m, m2);
            const float a1 = __builtin_amdgcn_exp2f((m - mn) * AB_C2), a2 = __builtin_amdgcn_exp2f((m2 - mn) * AB_C2);
            const float inv = 1.f / (l * a1 + l2 * a2);
            const float f1 = a1 * inv, f2 = a2 * inv;
            u16* dst = ctx + (size_t)(128 * qb + 32 * rg + r32) * 4096 + head * 128;
#pragma unroll
            for (int i = 0; i < 4; ++i)
#pragma unroll
                for (int g = 0; g < 4; ++g) {
                    float v[4];
#pragma unroll
                    for (int e = 0; e < 4; ++e) v[e] = o[i][4 * g + e] * f1 + mo[(rg * 64 + i * 16 + 4 * g + e) * 64 + lane] * f2;
                    u32x2 w = {cvtpk(v[0], v[1]), cvtpk(v[2], v[3])};
                    *(u32x2*)(dst + 32 * i + 8 * g + 4 * hi) = w;
                }
        }
        __syncthreads();
    }
}

constexpr int AA_LDS = 65536 + 69632 + 8 * 64 * 10 * 4;
__global__ __launch_bounds__(1024, 1) void attnA_kernel(const u16* __restrict__ Qa, const u16* __restrict__ Ka,
                                                        const u16* __restrict__ Vimg, u16* __restrict__ ctxA) {
    extern __shared__ __attribute__((aligned(16))) char lds[];
    const int tid = threadIdx.x, lane = tid & 63, wid = __builtin_amdgcn_readfirstlane(tid >> 6);
    const int r32 = lane & 31, hi = lane >> 5, tk = wid & 7, kh = wid >> 3;
#pragma unroll
    for (int i = 0; i < 4; ++i) { int p = tid + 1024 * i; glds16(Ka + p * 8, lds + p * 16); }
#pragma unroll
    for (int i = 0; i < 5; ++i) { int p = tid + 1024 * i; if (p < 4352) glds16(Vimg + p * 8, lds + 65536 + p * 16); }
    const int g = blockIdx.x * 8 + tk, head = g & 31, qblk = g >> 5;
    const int row = 32 * qblk + r32;
    constexpr float C2 = 0.25f * LOG2E;
    bf16x8 qf;
    {
        const u32x4 qr = *(const u32x4*)(Qa + (size_t)row * 512 + head * 16 + 8 * hi);
        u32x4 qs;
#pragma unroll
        for (int i = 0; i < 4; ++i)
            qs[i] = cvtpk(__uint_as_float(qr[i] << 16) * C2, __uint_as_float(qr[i] & 0xffff0000u) * C2);
        qf = __builtin_bit_cast(bf16x8, qs);
    }
    const int dd = r32 <= 16 ? r32 : (r32 & 15);
    const char* kp = lds + kh * 32768 + r32 * 32 + 16 * hi;
    const char* vp = lds + 65536 + kh * (64 * 544) + (dd * 2 + hi) * 16;
    f32x16 o = f32x16{};
    float m = 0.f;
    f32x16 negm = f32x16{};
    WAITV0();
    __syncthreads();
    for (int it = 0; it < 16; ++it) {
        bf16x8 k0 = *(const bf16x8*)(kp + it * 2048);
        bf16x8 k1 = *(const bf16x8*)(kp + it * 2048 + 1024);
        f32x16 s0 = __builtin_amdgcn_mfma_f32_32x32x16_bf16(k0, qf, negm, 0, 0, 0);
        f32x16 s1 = __builtin_amdgcn_mfma_f32_32x32x16_bf16(k1, qf, negm, 0, 0, 0);
        float pm = s0[0];
#pragma unroll
        for (int r = 1; r < 16; ++r) pm = fmaxf(pm, s0[r]);
#pragma unroll
        for (int r = 0; r < 16; ++r) pm = fmaxf(pm, s1[r]);
        pm = swapmax(pm);
        if (it == 0 || !__all(pm <= 8.f)) {
            const float dl = it == 0 ? pm : fmaxf(pm, 0.f);
            const float alpha = it == 0 ? 1.f : __builtin_amdgcn_exp2f(-dl);
            m += dl;
#pragma unroll
            for (int r = 0; r < 16; ++r) { s0[r] -= dl; s1[r] -= dl; negm[r] = -m; }
#pragma unroll
            for (int r = 0; r < 9; ++r) o[r] *= alpha;
        }
#pragma unroll
        for (int r = 0; r < 16; ++r) { s0[r] = __builtin_amdgcn_exp2f(s0[r]); s1[r] = __builtin_amdgcn_exp2f(s1[r]); }
#pragma unroll
        for (int s = 0; s < 2; ++s) {
            u32x4 w0 = {cvtpk(s0[8 * s], s0[8 * s + 1]), cvtpk(s0[8 * s + 2], s0[8 * s + 3]), cvtpk(s0[8 * s + 4], s0[8 * s + 5]), cvtpk(s0[8 * s + 6], s0[8 * s + 7])};
            u32x4 w1 = {cvtpk(s1[8 * s], s1[8 * s + 1]), cvtpk(s1[8 * s + 2], s1[8 * s + 3]), cvtpk(s1[8 * s + 4], s1[8 * s + 5]), cvtpk(s1[8 * s + 6], s1[8 * s + 7])};
            bf16x8 v0 = *(const bf16x8*)(vp + (4 * it + s) * 544);
            bf16x8 v1 = *(const bf16x8*)(vp + (4 * it + 2 + s) * 544);
            o = __builtin_amdgcn_mfma_f32_32x32x16_bf16(v0, __builtin_bit_cast(bf16x8, w0), o, 0, 0, 0);
            o = __builtin_amdgcn_mfma_f32_32x32x16_bf16(v1, __builtin_bit_cast(bf16x8, w1), o, 0, 0, 0);
        }
    }
    float* mg = (float*)(lds + 65536 + 69632) + tk * 640;
    if (kh == 1) {
#pragma unroll
        for (int r = 0; r < 9; ++r) mg[r * 64 + lane] = o[r];
        mg[9 * 64 + lane] = m;
    }
    __syncthreads();
    if (kh == 0) {
        const float m2 = mg[9 * 64 + lane];
        const float mn = fmaxf(m, m2);
        const float a1 = __builtin_amdgcn_exp2f(m - mn), a2 = __builtin_amdgcn_exp2f(m2 - mn);
        float ov[9];
#pragma unroll
        for (int r = 0; r < 9; ++r) ov[r] = o[r] * a1 + mg[r * 64 + lane] * a2;
        const float l = __shfl(ov[8], r32);
        const float inv = 1.f / l;
        u16* dst = ctxA + (size_t)row * 512 + head * 16 + 4 * hi;
#pragma unroll
        for (int g2 = 0; g2 < 2; ++g2) {
            u32x2 w = {cvtpk(ov[4 * g2] * inv, ov[4 * g2 + 1] * inv), cvtpk(ov[4 * g2 + 2] * inv, ov[4 * g2 + 3] * inv)};
            *(u32x2*)(dst + 8 * g2) = w;
        }
    }
}

extern "C" void kernel_launch(void* const* d_in, const int* in_sizes, int n_in,
                              void* d_out, int out_size, void* d_ws, size_t ws_size,
                              hipStream_t stream) {
    const u16* A_x = (const u16*)d_in[0];
    const u16* A_wq = (const u16*)d_in[1];
    const u16* A_wk = (const u16*)d_in[2];
    const u16* A_wv = (const u16*)d_in[3];
    const u16* A_wo = (const u16*)d_in[4];
    const u16* B_x = (const u16*)d_in[5];
    const u16* B_ckv = (const u16*)d_in[6];
    const u16* B_wq = (const u16*)d_in[7];
    const u16* B_wuk = (const u16*)d_in[8];
    const u16* B_wuv = (const u16*)d_in[9];
    const u16* B_wo = (const u16*)d_in[10];
    char* ws = (char*)d_ws;
    const size_t MB = 1 << 20;
    u16* QKV = (u16*)ws;
    u16* ctxB = (u16*)(ws + 48 * MB);
    u16* Qa = (u16*)(ws + 64 * MB);
    u16* ctxA = (u16*)(ws + 66 * MB);
    u16* Ka = (u16*)(ws + 68 * MB);
    u16* Vimg = (u16*)(ws + 68 * MB + 65536);
    float* part = (float*)(ws + 72 * MB);
    u16* out = (u16*)d_out;

    hipFuncSetAttribute((const void*)proj_kernel, hipFuncAttributeMaxDynamicSharedMemorySize, P_LDS);
    hipFuncSetAttribute((const void*)oproj_kernel, hipFuncAttributeMaxDynamicSharedMemorySize, O_LDS);
    hipFuncSetAttribute((const void*)attnB_kernel, hipFuncAttributeMaxDynamicSharedMemorySize, AB_LDS);
    hipFuncSetAttribute((const void*)attnA_kernel, hipFuncAttributeMaxDynamicSharedMemorySize, AA_LDS);

    proj_kernel<<<256, 512, P_LDS, stream>>>(A_x, A_wq, A_wk, A_wv, B_x, B_ckv, B_wq, B_wuk, B_wuv, QKV, Qa, Ka, Vimg);
    attnB_kernel<<<256, 512, AB_LDS, stream>>>(QKV, ctxB);
    attnA_kernel<<<256, 1024, AA_LDS, stream>>>(Qa, Ka, Vimg, ctxA);
    oproj_kernel<<<320, 512, O_LDS, stream>>>(ctxB, B_wo, ctxA, A_wo, part, out);
    reduce_kernel<<<1024, 256, 0, stream>>>(part, out);
}
```

```cpp
#include <hip/hip_runtime.h>
#include <hip/hip_bf16.h>
#include <stdint.h>

typedef short bf16x8 __attribute__((ext_vector_type(8)));
typedef short s16x4 __attribute__((ext_vector_type(4)));
typedef float f32x16 __attribute__((ext_vector_type(16)));
typedef float f32x4 __attribute__((ext_vector_type(4)));
typedef float f32x2 __attribute__((ext_vector_type(2)));
typedef unsigned u32x4 __attribute__((ext_vector_type(4)));
typedef unsigned u32x2 __attribute__((ext_vector_type(2)));
typedef unsigned short u16;
typedef __bf16 bf2_t __attribute__((ext_vector_type(2)));
typedef __attribute__((address_space(3))) s16x4* lds_s16x4_p;

constexpr int S = 2048;
constexpr float LOG2E = 1.4426950408889634f;

__device__ __forceinline__ unsigned cvtpk(float lo, float hi) {
    bf2_t v = {(__bf16)lo, (__bf16)hi}; return __builtin_bit_cast(unsigned, v);
}
__device__ __forceinline__ s16x4 trread(const char* p) {
    return __builtin_amdgcn_ds_read_tr16_b64_v4i16((lds_s16x4_p)(p));
}
__device__ __forceinline__ bf16x8 cat8(s16x4 a, s16x4 b) {
    return (bf16x8){a[0], a[1], a[2], a[3], b[0], b[1], b[2], b[3]};
}
__device__ __forceinline__ int swz256(int row) { return ((row & 3) << 2) | ((row >> 2) & 3); }
__device__ __forceinline__ int off_b(int row, int ch) { return 256 * row + 16 * (ch ^ swz256(row)); }
__device__ __forceinline__ void glds16(const void* g, char* l) {
    __builtin_amdgcn_global_load_lds((const unsigned*)g, (unsigned*)l, 16, 0, 0);
}
#define WAITV0() asm volatile("s_waitcnt vmcnt(0)" ::: "memory")
__device__ __forceinline__ float swapmax(float v) {
    auto rr = __builtin_amdgcn_permlane32_swap(__float_as_uint(v), __float_as_uint(v), false, false);
    return fmaxf(__uint_as_float(rr[0]), __uint_as_float(rr[1]));
}
__device__ __forceinline__ float swapsum(float v) {
    auto rr = __builtin_amdgcn_permlane32_swap(__float_as_uint(v), __float_as_uint(v), false, false);
    return __uint_as_float(rr[0]) + __uint_as_float(rr[1]);
}

template <int WM> struct GCfg { static constexpr int ABYTES = WM * 8192, STAGE = WM * 8192 + 16384, NT = 128 * WM, NLD = 4 + 8 / WM; };

template <bool KVA, int WM>
__device__ __forceinline__ void gemm_stage(char* buf, const u16* A, int lda, const u16* W, int ldw, const u16* W2,
                                           int m0, int n0, int k0, int tid) {
    constexpr int NT = GCfg<WM>::NT;
#pragma unroll
    for (int i = 0; i < 4; ++i) {
        int p = tid + NT * i;
        int row = p >> 3, slot = p & 7, ch = slot ^ ((row >> 1) & 7);
        glds16(A + (size_t)(m0 + row) * lda + k0 + ch * 8, buf + p * 16);
    }
#pragma unroll
    for (int i = 0; i < 8 / WM; ++i) {
        int p = tid + NT * i;
        int row = p >> 4, slot = p & 15, ch = slot ^ swz256(row);
        const u16* src;
        if (KVA) {
            src = (ch < 2) ? W + (size_t)(k0 + row) * 16 + ch * 8
                           : ((ch < 4) ? W2 + (size_t)(k0 + row) * 16 + (ch - 2) * 8 : W + (size_t)(k0 + row) * 16);
        } else {
            src = W + (size_t)(k0 + row) * ldw + n0 + ch * 8;
        }
        glds16(src, buf + GCfg<WM>::ABYTES + p * 16);
    }
}

template <bool KVA, int WM, int NS>
__device__ __forceinline__ void gemm_tile(char* lds, const u16* A, int lda, const u16* W, int ldw, const u16* W2,
                                          int m0, int n0, int kbeg, int nkt, f32x16 (&acc)[2][2]) {
    constexpr int STG = GCfg<WM>::STAGE, NLD = GCfg<WM>::NLD;
    const int tid = threadIdx.x, lane = tid & 63, wid = tid >> 6, wr = wid >> 1, wc = wid & 1;
    const int r32 = lane & 31, hi = lane >> 5;
#pragma unroll
    for (int i = 0; i < 2; ++i)
#pragma unroll
        for (int j = 0; j < 2; ++j) acc[i][j] = f32x16{};
#pragma unroll
    for (int i = 0; i < NS - 1; ++i) gemm_stage<KVA, WM>(lds + i * STG, A, lda, W, ldw, W2, m0, n0, kbeg + i * 64, tid);
    const unsigned lbase = (unsigned)(uintptr_t)lds;
    const int asw = ((wr * 64 + r32) >> 1) & 7;
    const int blk = (lane >> 4) & 1, q4 = (lane & 15) >> 2, p4 = lane & 3;
    unsigned xa0[4], wa0[4];
#pragma unroll
    for (int ks = 0; ks < 4; ++ks) xa0[ks] = lbase + 128 * (wr * 64 + r32) + 16 * ((2 * ks + hi) ^ asw);
#pragma unroll
    for (int i = 0; i < 4; ++i)
        wa0[i] = lbase + GCfg<WM>::ABYTES + off_b(8 * hi + 4 * (i & 1) + q4, 4 * (wc * 2 + (i >> 1)) + 2 * blk + (p4 >> 1)) + 8 * (p4 & 1);
#define G_RD(ks, X0, X1, W0, W1, W2, W3) do { \
        asm volatile("ds_read_b128 %0, %6\n\tds_read_b128 %1, %6 offset:4096\n\t" \
                     "ds_read_b64_tr_b16 %2, %7 offset:%11\n\tds_read_b64_tr_b16 %3, %8 offset:%11\n\t" \
                     "ds_read_b64_tr_b16 %4, %9 offset:%11\n\tds_read_b64_tr_b16 %5, %10 offset:%11" \
                     : "=&v"(X0), "=&v"(X1), "=&v"(W0), "=&v"(W1), "=&v"(W2), "=&v"(W3) \
                     : "v"(xa[ks]), "v"(wa[0]), "v"(wa[1]), "v"(wa[2]), "v"(wa[3]), "i"(4096 * (ks)) : "memory"); } while (0)
#define G_WAIT(n, X0, X1, W0, W1, W2, W3) \
        asm volatile("s_waitcnt lgkmcnt(" #n ")" : "+v"(X0), "+v"(X1), "+v"(W0), "+v"(W1), "+v"(W2), "+v"(W3) :: "memory")
#define G_MMA(X0, X1, W0, W1, W2, W3) do { \
        bf16x8 w0_ = cat8(W0, W1), w1_ = cat8(W2, W3); \
        acc[0][0] = __builtin_amdgcn_mfma_f32_32x32x16_bf16(w0_, X0, acc[0][0], 0, 0, 0); \
        acc[0][1] = __builtin_amdgcn_mfma_f32_32x32x16_bf16(w1_, X0, acc[0][1], 0, 0, 0); \
        acc[1][0] = __builtin_amdgcn_mfma_f32_32x32x16_bf16(w0_, X1, acc[1][0], 0, 0, 0); \
        acc[1][1] = __builtin_amdgcn_mfma_f32_32x32x16_bf16(w1_, X1, acc[1][1], 0, 0, 0); } while (0)
    int cb = 0;
    for (int t = 0; t < nkt; ++t) {
        const int rem = nkt - 1 - t;
        if (NS >= 5 && rem >= 3) asm volatile("s_waitcnt vmcnt(%0)" :: "i"(3 * NLD) : "memory");
        else if (NS >= 4 && rem >= 2) asm volatile("s_waitcnt vmcnt(%0)" :: "i"(2 * NLD) : "memory");
        else if (NS >= 3 && rem >= 1) asm volatile("s_waitcnt vmcnt(%0)" :: "i"(NLD) : "memory");
        else WAITV0();
        __builtin_amdgcn_s_barrier();
        asm volatile("" ::: "memory");
        const unsigned bo = cb * STG;
        unsigned xa[4], wa[4];
#pragma unroll
        for (int i = 0; i < 4; ++i) { xa[i] = xa0[i] + bo; wa[i] = wa0[i] + bo; }
        bf16x8 xA0, xA1, xB0, xB1; s16x4 a0, a1, a2, a3, b0, b1, b2, b3;
        G_RD(0, xA0, xA1, a0, a1, a2, a3);
        G_RD(1, xB0, xB1, b0, b1, b2, b3);
        if (t + NS - 1 < nkt) {
            int nb = cb + NS - 1; if (nb >= NS) nb -= NS;
            gemm_stage<KVA, WM>(lds + nb * STG, A, lda, W, ldw, W2, m0, n0, kbeg + (t + NS - 1) * 64, tid);
        }
        G_WAIT(6, xA0, xA1, a0, a1, a2, a3);
        G_MMA(xA0, xA1, a0, a1, a2, a3);
        G_RD(2, xA0, xA1, a0, a1, a2, a3);
        G_WAIT(6, xB0, xB1, b0, b1, b2, b3);
        G_MMA(xB0, xB1, b0, b1, b2, b3);
        G_RD(3, xB0, xB1, b0, b1, b2, b3);
        G_WAIT(6, xA0, xA1, a0, a1, a2, a3);
        G_MMA(xA0, xA1, a0, a1, a2, a3);
        G_WAIT(0, xB0, xB1, b0, b1, b2, b3);
        G_MMA(xB0, xB1, b0, b1, b2, b3);
        if (++cb == NS) cb = 0;
    }
}

__device__ __forceinline__ void store_bf16_tile(u16* dst, int ldd, f32x16 (&acc)[2][2]) {
    const int tid = threadIdx.x, lane = tid & 63, wid = tid >> 6, wr = wid >> 1, wc = wid & 1;
    const int r32 = lane & 31, hi = lane >> 5;
#pragma unroll
    for (int mi = 0; mi < 2; ++mi)
#pragma unroll
        for (int ni = 0; ni < 2; ++ni)
#pragma unroll
            for (int g = 0; g < 4; ++g) {
                u32x2 v = {cvtpk(acc[mi][ni][4 * g], acc[mi][ni][4 * g + 1]), cvtpk(acc[mi][ni][4 * g + 2], acc[mi][ni][4 * g + 3])};
                *(u32x2*)(dst + (size_t)(wr * 64 + mi * 32 + r32) * ldd + wc * 64 + ni * 32 + 8 * g + 4 * hi) = v;
            }
}

constexpr int P_STG = 65536, P_LDS = 2 * P_STG + 32768;
struct PTile { const u16* A; const u16* W; const u16* W2; u16* dst; int ldw, ldd, m0, n0, kind, n0u; };
__device__ __forceinline__ PTile p_tile(int cbk, int u0, int nu, const u16* xin, const u16* wqa, const u16* wka, const u16* wva, const u16* xb,
                                        const u16* ckv, const u16* wq, const u16* wuk, const u16* wuv, u16* QKV, u16* Qa) {
    PTile t;
    const int m0 = u0 * 32;
    t.m0 = m0; t.n0u = (nu + 1) >> 1;
    if (cbk < 48) {
        const int pj = cbk >> 4, nt = cbk & 15;
        t.A = pj == 0 ? xb : ckv; t.W = pj == 0 ? wq : (pj == 1 ? wuk : wuv); t.W2 = t.W; t.ldw = 4096; t.n0 = nt * 256;
        t.dst = QKV + (size_t)pj * (32 * S * 128) + ((size_t)nt * 2 * S + m0) * 128; t.ldd = 128; t.kind = 0;
    } else if (cbk < 50) {
        const int nt = cbk - 48;
        t.A = xin; t.W = wqa; t.W2 = wqa; t.ldw = 512; t.n0 = nt * 256;
        t.dst = Qa + (size_t)m0 * 512 + nt * 256; t.ldd = 512; t.kind = 1;
    } else {
        t.A = xin; t.W = wka; t.W2 = wva; t.ldw = 16; t.n0 = 0; t.dst = Qa; t.ldd = 512; t.kind = 2;
    }
    return t;
}
__device__ __forceinline__ void p_stage(char* buf, const PTile& t, int k0, int tid, int wr) {
    {
        const char* base = (const char*)(t.A + (size_t)t.m0 * 512 + k0);
        const int row = tid >> 3;
        const unsigned voff = (unsigned)((row * 512 + ((tid & 7) ^ ((row >> 1) & 7)) * 8) * 2);
        const unsigned r1 = (unsigned)t.n0u * 32768u;
        glds16(base + voff, buf + tid * 16);
        if (wr == 0 || t.n0u == 4) glds16(base + (voff + 65536u), buf + (tid + 512) * 16);
        glds16(base + (voff + r1), buf + (tid + 1024) * 16);
        if (wr == 0) glds16(base + (voff + r1 + 65536u), buf + (tid + 1536) * 16);
    }
    const int row = tid >> 4, ch = (tid & 15) ^ swz256(row);
    if (t.kind != 2) {
        const char* base = (const char*)(t.W + (size_t)k0 * t.ldw + t.n0);
        const unsigned voff = (unsigned)((row * t.ldw + ch * 8) * 2), step = (unsigned)(64 * t.ldw);
#pragma unroll
        for (int i = 0; i < 4; ++i)
            glds16(base + (voff + (i & 1) * step + (i >> 1) * 256u), buf + 32768 + (i >> 1) * 16384 + (tid + 512 * (i & 1)) * 16);
    } else {
#pragma unroll
        for (int i = 0; i < 4; ++i) {
            const int r2 = row + 32 * (i & 1);
            const u16* src = ((i >> 1) == 0 && ch >= 2 && ch < 4) ? t.W2 + (size_t)(k0 + r2) * 16 + (ch - 2) * 8
                                                                  : t.W + (size_t)(k0 + r2) * 16 + (ch & 1) * 8;
            glds16(src, buf + 32768 + (i >> 1) * 16384 + (tid + 512 * (i & 1)) * 16);
        }
    }
}
template <int N> struct IC { static constexpr int value = N; };

__global__ __launch_bounds__(512, 1) void proj_kernel(const u16* __restrict__ xin, const u16* __restrict__ wqa, const u16* __restrict__ wka,
                                                      const u16* __restrict__ wva, const u16* __restrict__ xb, const u16* __restrict__ ckv,
                                                      const u16* __restrict__ wq, const u16* __restrict__ wuk, const u16* __restrict__ wuv,
                                                      u16* __restrict__ QKV, u16* __restrict__ Qa, u16* __restrict__ Ka, u16* __restrict__ Vimg) {
    extern __shared__ __attribute__((aligned(16))) char lds[];
    const int tid = threadIdx.x, lane = tid & 63, wid = __builtin_amdgcn_readfirstlane(tid >> 6), wr = wid >> 2, wc = wid & 3;
    const int r32 = lane & 31, hi = lane >> 5;
    const int b = blockIdx.x, c = (b & 7) * 32 + (b >> 3);
    if (c == 255) return;
    const int cbk = c / 5, pr = c - cbk * 5;
    const int nu0 = pr < 4 ? 7 : 6;
    PTile cur = p_tile(cbk, pr * 13, nu0, xin, wqa, wka, wva, xb, ckv, wq, wuk, wuv, QKV, Qa);
    const PTile nxt = p_tile(cbk, pr * 13 + nu0, 6, xin, wqa, wka, wva, xb, ckv, wq, wuk, wuv, QKV, Qa);
    p_stage(lds, cur, 0, tid, wr);
    const unsigned lbase = (unsigned)(uintptr_t)lds;
    const int asw = (r32 >> 1) & 7;
    const int blk = (lane >> 4) & 1, q4 = (lane & 15) >> 2, p4 = lane & 3;
    unsigned xa[4], wa[4];
#pragma unroll
    for (int ks = 0; ks < 4; ++ks) xa[ks] = lbase + 128 * (wr * 128 + r32) + 16 * ((2 * ks + hi) ^ asw);
#pragma unroll
    for (int i = 0; i < 4; ++i)
        wa[i] = lbase + 32768 + (wc >> 1) * 16384 + off_b(8 * hi + 4 * (i & 1) + q4, 4 * ((wc & 1) * 2 + (i >> 1)) + 2 * blk + (p4 >> 1)) + 8 * (p4 & 1);
#define P_RD4(ks, X, Wf) asm volatile( \
        "ds_read_b128 %0, %8\n\tds_read_b128 %1, %8 offset:4096\n\tds_read_b128 %2, %8 offset:8192\n\tds_read_b128 %3, %8 offset:12288\n\t" \
        "ds_read_b64_tr_b16 %4, %9 offset:%13\n\tds_read_b64_tr_b16 %5, %10 offset:%13\n\t" \
        "ds_read_b64_tr_b16 %6, %11 offset:%13\n\tds_read_b64_tr_b16 %7, %12 offset:%13" \
        : "=&v"(X[0]), "=&v"(X[1]), "=&v"(X[2]), "=&v"(X[3]), "=&v"(Wf[0]), "=&v"(Wf[1]), "=&v"(Wf[2]), "=&v"(Wf[3]) \
        : "v"(xa[ks]), "v"(wa[0]), "v"(wa[1]), "v"(wa[2]), "v"(wa[3]), "i"(4096 * (ks)) : "memory")
#define P_RD3(ks, X, Wf) asm volatile( \
        "ds_read_b128 %0, %7\n\tds_read_b128 %1, %7 offset:4096\n\tds_read_b128 %2, %7 offset:8192\n\t" \
        "ds_read_b64_tr_b16 %3, %8 offset:%12\n\tds_read_b64_tr_b16 %4, %9 offset:%12\n\t" \
        "ds_read_b64_tr_b16 %5, %10 offset:%12\n\tds_read_b64_tr_b16 %6, %11 offset:%12" \
        : "=&v"(X[0]), "=&v"(X[1]), "=&v"(X[2]), "=&v"(Wf[0]), "=&v"(Wf[1]), "=&v"(Wf[2]), "=&v"(Wf[3]) \
        : "v"(xa[ks]), "v"(wa[0]), "v"(wa[1]), "v"(wa[2]), "v"(wa[3]), "i"(4096 * (ks)) : "memory")
#define P_RD(ks, X, Wf) do { if constexpr (NU == 4) { P_RD4(ks, X, Wf); } else { P_RD3(ks, X, Wf); } } while (0)
#define P_WAIT(last, X, Wf) do { if constexpr (NU == 4) { \
            asm volatile("s_waitcnt lgkmcnt(%8)" : "+v"(X[0]), "+v"(X[1]), "+v"(X[2]), "+v"(X[3]), "+v"(Wf[0]), "+v"(Wf[1]), "+v"(Wf[2]), "+v"(Wf[3]) : "i"((last) ? 0 : 8) : "memory"); \
        } else { \
            asm volatile("s_waitcnt lgkmcnt(%7)" : "+v"(X[0]), "+v"(X[1]), "+v"(X[2]), "+v"(Wf[0]), "+v"(Wf[1]), "+v"(Wf[2]), "+v"(Wf[3]) : "i"((last) ? 0 : 7) : "memory"); \
        } } while (0)
#define P_MMA(X, Wf) do { bf16x8 w0_ = cat8(Wf[0], Wf[1]), w1_ = cat8(Wf[2], Wf[3]); \
        _Pragma("unroll") for (int mi = 0; mi < NU; ++mi) { \
            acc[mi][0] = __builtin_amdgcn_mfma_f32_32x32x16_bf16(w0_, X[mi], acc[mi][0], 0, 0, 0); \
            acc[mi][1] = __builtin_amdgcn_mfma_f32_32x32x16_bf16(w1_, X[mi], acc[mi][1], 0, 0, 0); } } while (0)
    const int prevu = wr == 0 ? cur.n0u : 3;
    auto run = [&](auto nuc, const int tl) __attribute__((always_inline)) {
        constexpr int NU = decltype(nuc)::value;
        f32x16 acc[NU][2];
#pragma unroll
        for (int i = 0; i < NU; ++i) { acc[i][0] = f32x16{}; acc[i][1] = f32x16{}; }
#pragma unroll 1
        for (int kt = 0; kt < 8; ++kt) {
            if (tl == 1 && kt == 0 && cur.kind != 2) {
                if (prevu == 4) asm volatile("s_waitcnt vmcnt(16)" ::: "memory"); else asm volatile("s_waitcnt vmcnt(12)" ::: "memory");
            } else WAITV0();
            __builtin_amdgcn_s_barrier();
            asm volatile("" ::: "memory");
            bf16x8 xA[4], xB[4]; s16x4 wA[4], wB[4];
            P_RD(0, xA, wA);
            P_RD(1, xB, wB);
            if (kt < 7) p_stage(lds + ((kt + 1) & 1) * P_STG, cur, (kt + 1) * 64, tid, wr);
            else if (tl == 0) p_stage(lds, nxt, 0, tid, wr);
            P_WAIT(0, xA, wA); P_MMA(xA, wA);
            P_RD(2, xA, wA);
            P_WAIT(0, xB, wB); P_MMA(xB, wB);
            P_RD(3, xB, wB);
            P_WAIT(0, xA, wA); P_MMA(xA, wA);
            P_WAIT(1, xB, wB); P_MMA(xB, wB);
#pragma unroll
            for (int i = 0; i < 4; ++i) { xa[i] ^= (unsigned)P_STG; wa[i] ^= (unsigned)P_STG; }
        }
        int lz = lane; asm volatile("" : "+v"(lz));
        const int r32e = lz & 31, hie = lz >> 5;
        const int roff = wr * cur.n0u * 32;
        if (cur.kind != 2) {
            char* ep = lds + 2 * P_STG + wid * 4096;
            u16* dw = cur.kind == 0 ? cur.dst + (size_t)(wc >> 1) * (S * 128) + (wc & 1) * 64 : cur.dst + wc * 64;
#pragma unroll
            for (int mi = 0; mi < NU; ++mi) {
#pragma unroll
                for (int ni = 0; ni < 2; ++ni)
#pragma unroll
                    for (int g = 0; g < 4; ++g) {
                        u32x2 v = {cvtpk(acc[mi][ni][4 * g], acc[mi][ni][4 * g + 1]), cvtpk(acc[mi][ni][4 * g + 2], acc[mi][ni][4 * g + 3])};
                        *(u32x2*)(ep + r32e * 128 + 16 * ((ni * 4 + g) ^ (r32e & 7)) + 8 * hie) = v;
                    }
                asm volatile("s_waitcnt lgkmcnt(0)" ::: "memory");
#pragma unroll
                for (int i = 0; i < 4; ++i) {
                    const int idx = lz + 64 * i, row = idx >> 3, ch = idx & 7;
                    u32x4 v = *(const u32x4*)(ep + row * 128 + 16 * (ch ^ (row & 7)));
                    *(u32x4*)(dw + (size_t)(roff + mi * 32 + row) * cur.ldd + ch * 8) = v;
                }
                asm volatile("s_waitcnt lgkmcnt(0)" ::: "memory");
            }
        } else if (wc == 0) {
#pragma unroll
            for (int mi = 0; mi < NU; ++mi) {
                const int m = cur.m0 + roff + mi * 32 + r32e;
#pragma unroll
                for (int g = 0; g < 2; ++g) {
                    u32x2 v = {cvtpk(acc[mi][0][4 * g], acc[mi][0][4 * g + 1]), cvtpk(acc[mi][0][4 * g + 2], acc[mi][0][4 * g + 3])};
                    *(u32x2*)(Ka + (size_t)m * 16 + 8 * g + 4 * hie) = v;
                }
                const int s = m >> 4, kk = m & 15, hh = (kk >> 2) & 1, j = (kk & 3) + 4 * (kk >> 3);
#pragma unroll
                for (int r = 8; r < 16; ++r) {
                    const int d = (r & 3) + 8 * ((r >> 2) - 2) + 4 * hie;
                    Vimg[((s * 17 + d) * 2 + hh) * 8 + j] = (u16)(cvtpk(acc[mi][0][r], 0.f) & 0xffff);
                }
                if (hie == 0) Vimg[((s * 17 + 16) * 2 + hh) * 8 + j] = (u16)0x3F80;
            }
        }
    };
    if (prevu == 4) run(IC<4>{}, 0); else run(IC<3>{}, 0);
    cur = nxt;
    run(IC<3>{}, 1);
}

constexpr int O_NS = 2, O_STG = 32768, O_LDS = O_NS * O_STG;
__device__ __forceinline__ void o_stage(char* buf, const u16* A, int lda, const u16* W, int m0, int n0, int k0, int tid) {
    {
        const char* base = (const char*)(A + (size_t)m0 * lda + k0);
        const int row = tid >> 3;
        const unsigned voff = (unsigned)((row * lda + ((tid & 7) ^ ((row >> 1) & 7)) * 8) * 2), step = (unsigned)(128 * lda);
#pragma unroll
        for (int i = 0; i < 2; ++i) glds16(base + (voff + step * i), buf + (tid + 512 * i) * 16);
    }
    {
        const char* base = (const char*)(W + (size_t)k0 * 512 + n0);
        const int row = tid >> 4;
        const unsigned voff = (unsigned)((row * 512 + ((tid & 15) ^ swz256(row)) * 8) * 2);
#pragma unroll
        for (int i = 0; i < 2; ++i) glds16(base + (voff + 32768u * i), buf + 16384 + (tid + 512 * i) * 16);
    }
}
__global__ __launch_bounds__(512, 2) void oproj_kernel(const u16* __restrict__ ctxB, const u16* __restrict__ wo_b,
                                                       const u16* __restrict__ ctxA, const u16* __restrict__ wo_a,
                                                       float* __restrict__ part, u16* __restrict__ out) {
    extern __shared__ __attribute__((aligned(16))) char lds[];
    const int b = blockIdx.x;
    const int tid = threadIdx.x, lane = tid & 63, wid = __builtin_amdgcn_readfirstlane(tid >> 6);
    const int wr = (wid >> 1) & 1, wc = wid & 1, kg = wid >> 2;
    const int r32 = lane & 31, hi = lane >> 5;
    const bool isB = b < 256;
    const int sp = b & 3, nt = isB ? (b >> 2) & 3 : (b & 3), mt = isB ? b >> 4 : (b - 256) >> 2;
    const u16* A = isB ? ctxB : ctxA; const u16* W = isB ? wo_b : wo_a;
    const int lda = isB ? 4096 : 512, kbeg = isB ? sp * 1024 : 0, nkt = isB ? 16 : 8;
    const int m0 = mt * 128, n0 = nt * 128;
#pragma unroll
    for (int i = 0; i < O_NS - 1; ++i) o_stage(lds + i * O_STG, A, lda, W, m0, n0, kbeg + i * 64, tid);
    const unsigned lbase = (unsigned)(uintptr_t)lds;
    const int asw = (r32 >> 1) & 7;
    const int blk = (lane >> 4) & 1, q4 = (lane & 15) >> 2, p4 = lane & 3;
    unsigned xa0[2], wa0[4];
#pragma unroll
    for (int k2 = 0; k2 < 2; ++k2) xa0[k2] = lbase + 128 * (wr * 64 + r32) + 16 * ((2 * (2 * kg + k2) + hi) ^ asw);
#pragma unroll
    for (int i = 0; i < 4; ++i)
        wa0[i] = lbase + 16384 + kg * 8192 + off_b(8 * hi + 4 * (i & 1) + q4, 4 * (wc * 2 + (i >> 1)) + 2 * blk + (p4 >> 1)) + 8 * (p4 & 1);
    f32x16 acc[2][2];
#pragma unroll
    for (int i = 0; i < 2; ++i) { acc[i][0] = f32x16{}; acc[i][1] = f32x16{}; }
    int cb = 0;
    for (int t = 0; t < nkt; ++t) {
        const int rem = nkt - 1 - t;
        if (O_NS >= 4 && rem >= 2) asm volatile("s_waitcnt vmcnt(8)" ::: "memory");
        else if (O_NS >= 3 && rem >= 1) asm volatile("s_waitcnt vmcnt(4)" ::: "memory");
        else WAITV0();
        __builtin_amdgcn_s_barrier();
        asm volatile("" ::: "memory");
        const unsigned bo = cb * O_STG;
        unsigned xa[4], wa[4];
        xa[0] = xa0[0] + bo; xa[1] = xa0[1] + bo; xa[2] = xa[3] = 0;
#pragma unroll
        for (int i = 0; i < 4; ++i) wa[i] = wa0[i] + bo;
        bf16x8 xA0, xA1, xB0, xB1; s16x4 a0, a1, a2, a3, b0, b1, b2, b3;
        G_RD(0, xA0, xA1, a0, a1, a2, a3);
        G_RD(1, xB0, xB1, b0, b1, b2, b3);
        if (t + O_NS - 1 < nkt) {
            int nb = cb + O_NS - 1; if (nb >= O_NS) nb -= O_NS;
            o_stage(lds + nb * O_STG, A, lda, W, m0, n0, kbeg + (t + O_NS - 1) * 64, tid);
        }
        G_WAIT(6, xA0, xA1, a0, a1, a2, a3);
        G_MMA(xA0, xA1, a0, a1, a2, a3);
        G_WAIT(0, xB0, xB1, b0, b1, b2, b3);
        G_MMA(xB0, xB1, b0, b1, b2, b3);
        if (++cb == O_NS) cb = 0;
    }
    __syncthreads();
    float* red = (float*)lds;
    if (kg == 1) {
#pragma unroll
        for (int mi = 0; mi < 2; ++mi)
#pragma unroll
            for (int ni = 0; ni < 2; ++ni)
#pragma unroll
                for (int r = 0; r < 16; ++r) red[(((wid & 3) * 4 + mi * 2 + ni) * 16 + r) * 64 + lane] = acc[mi][ni][r];
    }
    __syncthreads();
    if (kg == 0) {
#pragma unroll
        for (int mi = 0; mi < 2; ++mi)
#pragma unroll
            for (int ni = 0; ni < 2; ++ni)
#pragma unroll
                for (int r = 0; r < 16; ++r) acc[mi][ni][r] += red[((wid * 4 + mi * 2 + ni) * 16 + r) * 64 + lane];
        if (isB) {
            float* dst = part + (size_t)sp * S * 512;
#pragma unroll
            for (int mi = 0; mi < 2; ++mi)
#pragma unroll
                for (int ni = 0; ni < 2; ++ni)
#pragma unroll
                    for (int g = 0; g < 4; ++g) {
                        f32x4 v = {acc[mi][ni][4 * g], acc[mi][ni][4 * g + 1], acc[mi][ni][4 * g + 2], acc[mi][ni][4 * g + 3]};
                        *(f32x4*)(dst + (size_t)(m0 + wr * 64 + mi * 32 + r32) * 512 + n0 + wc * 64 + ni * 32 + 8 * g + 4 * hi) = v;
                    }
        } else {
#pragma unroll
            for (int mi = 0; mi < 2; ++mi)
#pragma unroll
                for (int ni = 0; ni < 2; ++ni)
#pragma unroll
                    for (int g = 0; g < 4; ++g) {
                        u32x2 v = {cvtpk(acc[mi][ni][4 * g], acc[mi][ni][4 * g + 1]), cvtpk(acc[mi][ni][4 * g + 2], acc[mi][ni][4 * g + 3])};
                        *(u32x2*)(out + (size_t)(m0 + wr * 64 + mi * 32 + r32) * 1024 + n0 + wc * 64 + ni * 32 + 8 * g + 4 * hi) = v;
                    }
        }
    }
}

__global__ __launch_bounds__(256) void reduce_kernel(const float* __restrict__ part, u16* __restrict__ out) {
    const int i = blockIdx.x * 256 + threadIdx.x;
    const f32x4* p = (const f32x4*)part;
    f32x4 a = p[i], b2 = p[i + 262144], c = p[i + 2 * 262144], d = p[i + 3 * 262144];
    f32x4 s = (a + b2) + (c + d);
    const int m = i >> 7, n = (i & 127) * 4;
    u32x2 v = {cvtpk(s[0], s[1]), cvtpk(s[2], s[3])};
    *(u32x2*)(out + (size_t)m * 1024 + 512 + n) = v;
}

constexpr int AB_LDS = 163840;

__device__ __forceinline__ void ab_stage1(char* buf, const u16* P, int t, int tid) {
    const char* base = (const char*)(P + (size_t)t * 128 * 128);
    const unsigned voff = (unsigned)(((tid >> 4) * 128 + ((tid & 15) ^ swz256(tid >> 4)) * 8) * 2);
#pragma unroll
    for (int i = 0; i < 4; ++i) glds16(base + (voff + 8192u * i), buf + (tid + 512 * i) * 16);
}
__device__ __forceinline__ void ab_qk(f32x16& s0, f32x16& s1, const char* kb, const char* ql, int kh, int rg, int r32, int hi) {
    s0 = f32x16{}; s1 = f32x16{};
#pragma unroll
    for (int d0 = 0; d0 < 8; ++d0) {
        bf16x8 qf = *(const bf16x8*)(ql + off_b(32 * rg + r32, 2 * d0 + hi));
        bf16x8 k0 = *(const bf16x8*)(kb + off_b(64 * kh + r32, 2 * d0 + hi));
        bf16x8 k1 = *(const bf16x8*)(kb + off_b(64 * kh + 32 + r32, 2 * d0 + hi));
        s0 = __builtin_amdgcn_mfma_f32_32x32x16_bf16(k0, qf, s0, 0, 0, 0);
        s1 = __builtin_amdgcn_mfma_f32_32x32x16_bf16(k1, qf, s1, 0, 0, 0);
    }
}
__device__ __forceinline__ void ab_mask(f32x16& s0, f32x16& s1, int dq) {
    const float NEG = -__builtin_inff();
#pragma unroll
    for (int r = 0; r < 16; ++r) {
        const int c = (r & 3) + 8 * (r >> 2);
        if (c > dq) s0[r] = NEG;
        if (c + 32 > dq) s1[r] = NEG;
    }
}
__device__ __forceinline__ float ab_max(const f32x16& s0, const f32x16& s1) {
    float pm = s0[0];
#pragma unroll
    for (int r = 1; r < 16; ++r) pm = fmaxf(pm, s0[r]);
#pragma unroll
    for (int r = 0; r < 16; ++r) pm = fmaxf(pm, s1[r]);
    return swapmax(pm);
}
constexpr float AB_C2 = 0.08838834764831845f * LOG2E;
__device__ __forceinline__ void ab_exp(f32x16& s0, f32x16& s1, float m, float& l, bf16x8 (&pf)[4]) {
    const float mc = -m * AB_C2;
    float ps = 0.f;
#pragma unroll
    for (int r = 0; r < 16; ++r) { s0[r] = __builtin_amdgcn_exp2f(fmaf(s0[r], AB_C2, mc)); ps += s0[r]; }
#pragma unroll
    for (int r = 0; r < 16; ++r) { s1[r] = __builtin_amdgcn_exp2f(fmaf(s1[r], AB_C2, mc)); ps += s1[r]; }
    l += ps;
#pragma unroll
    for (int s = 0; s < 2; ++s) {
        u32x4 w0 = {cvtpk(s0[8 * s], s0[8 * s + 1]), cvtpk(s0[8 * s + 2], s0[8 * s + 3]), cvtpk(s0[8 * s + 4], s0[8 * s + 5]), cvtpk(s0[8 * s + 6], s0[8 * s + 7])};
        u32x4 w1 = {cvtpk(s1[8 * s], s1[8 * s + 1]), cvtpk(s1[8 * s + 2], s1[8 * s + 3]), cvtpk(s1[8 * s + 4], s1[8 * s + 5]), cvtpk(s1[8 * s + 6], s1[8 * s + 7])};
        pf[s] = __builtin_bit_cast(bf16x8, w0); pf[2 + s] = __builtin_bit_cast(bf16x8, w1);
    }
}
__device__ __forceinline__ void ab_pv(f32x16 (&o)[4], const unsigned (&va)[8], const bf16x8 (&pf)[4]) {
#define V_RD(ks, h, R) asm volatile( \
        "ds_read_b64_tr_b16 %0, %4 offset:%8\n\tds_read_b64_tr_b16 %1, %5 offset:%8\n\t" \
        "ds_read_b64_tr_b16 %2, %6 offset:%8\n\tds_read_b64_tr_b16 %3, %7 offset:%8" \
        : "=&v"(R[0]), "=&v"(R[1]), "=&v"(R[2]), "=&v"(R[3]) \
        : "v"(va[4 * h]), "v"(va[4 * h + 1]), "v"(va[4 * h + 2]), "v"(va[4 * h + 3]), "i"(4096 * (ks)))
#define V_WAIT(n, R) asm volatile("s_waitcnt lgkmcnt(" #n ")" : "+v"(R[0]), "+v"(R[1]), "+v"(R[2]), "+v"(R[3]))
#define V_MMA(ks, h, R) do { \
        o[2 * h] = __builtin_amdgcn_mfma_f32_32x32x16_bf16(cat8(R[0], R[1]), pf[ks], o[2 * h], 0, 0, 0); \
        o[2 * h + 1] = __builtin_amdgcn_mfma_f32_32x32x16_bf16(cat8(R[2], R[3]), pf[ks], o[2 * h + 1], 0, 0, 0); } while (0)
    s16x4 ra[4], rb[4];
    V_RD(0, 0, ra); V_RD(0, 1, rb);
    V_WAIT(4, ra); V_MMA(0, 0, ra); V_RD(1, 0, ra);
    V_WAIT(4, rb); V_MMA(0, 1, rb); V_RD(1, 1, rb);
    V_WAIT(4, ra); V_MMA(1, 0, ra); V_RD(2, 0, ra);
    V_WAIT(4, rb); V_MMA(1, 1, rb); V_RD(2, 1, rb);
    V_WAIT(4, ra); V_MMA(2, 0, ra); V_RD(3, 0, ra);
    V_WAIT(4, rb); V_MMA(2, 1, rb); V_RD(3, 1, rb);
    V_WAIT(4, ra); V_MMA(3, 0, ra);
    V_WAIT(0, rb); V_MMA(3, 1, rb);
}
__device__ __forceinline__ void ab_rescale(float pm, float& m, float& l, f32x16 (&o)[4]) {
    if (!__all((pm - m) * AB_C2 <= 8.f)) {
        const float mn = fmaxf(m, pm);
        const float alpha = __builtin_amdgcn_exp2f((m - mn) * AB_C2);
        m = mn; l *= alpha;
#pragma unroll
        for (int i = 0; i < 4; ++i)
#pragma unroll
            for (int r = 0; r < 16; ++r) o[i][r] *= alpha;
    }
}

__global__ __launch_bounds__(512, 1) void attnB_kernel(const u16* __restrict__ QKV, u16* __restrict__ ctx) {
    extern __shared__ __attribute__((aligned(16))) char lds[];
    const int tid = threadIdx.x, lane = tid & 63, wid = __builtin_amdgcn_readfirstlane(tid >> 6);
    const int rg = wid & 3, kh = wid >> 2, r32 = lane & 31, hi = lane >> 5;
    const int b = blockIdx.x, jj = b >> 3, head = (b & 7) * 4 + (jj >> 3), x = jj & 7;
    const u16* Qh = QKV + (size_t)head * S * 128;
    const u16* Kh = QKV + (size_t)32 * S * 128 + (size_t)head * S * 128;
    const u16* Vh = QKV + (size_t)64 * S * 128 + (size_t)head * S * 128;
    const int blk = (lane >> 4) & 1, q4 = (lane & 15) >> 2, p4 = lane & 3;
    char* Kl = lds; char* Vl = lds + 65536; char* Ql = lds + 131072;
    unsigned va0[8];
#pragma unroll
    for (int i = 0; i < 8; ++i)
        va0[i] = (unsigned)(uintptr_t)Vl + off_b(64 * kh + 4 * hi + 8 * (i & 1) + q4, 4 * (i >> 1) + 2 * blk + (p4 >> 1)) + 8 * (p4 & 1);
    const int dq = 32 * rg + r32 - 64 * kh - 4 * hi;
    for (int pass = 0; pass < 2; ++pass) {
        const int qb = pass == 0 ? 15 - x : x;
        const int nt = qb + 1;
        int tz = tid; asm volatile("" : "+v"(tz));
        ab_stage1(Kl, Kh, 0, tz);
        ab_stage1(Vl, Vh, 0, tz);
        if (nt > 1) ab_stage1(Kl + 32768, Kh, 1, tz);
        ab_stage1(Ql, Qh, qb, tz);
        f32x16 o[4];
#pragma unroll
        for (int i = 0; i < 4; ++i) o[i] = f32x16{};
        float l = 0.f;
        WAITV0();
        __syncthreads();
        f32x16 s0, s1;
        ab_qk(s0, s1, Kl, Ql, kh, rg, r32, hi);
        if (nt == 1) ab_mask(s0, s1, dq);
        float m = fmaxf(ab_max(s0, s1), -1e30f);
        for (int t = 0; t < nt - 1; ++t) {
            WAITV0();
            __syncthreads();
            if (t + 2 < nt) ab_stage1(Kl + (t & 1) * 32768, Kh, t + 2, tid);
            ab_stage1(Vl + ((t + 1) & 1) * 32768, Vh, t + 1, tid);
            f32x16 n0, n1;
            bf16x8 pf[4];
            if (kh == 0) {
                ab_qk(n0, n1, Kl + ((t + 1) & 1) * 32768, Ql, kh, rg, r32, hi);
                __builtin_amdgcn_sched_barrier(0);
                ab_exp(s0, s1, m, l, pf);
                __builtin_amdgcn_sched_barrier(0);
                ab_pv(o, va0, pf);
            } else {
                ab_exp(s0, s1, m, l, pf);
                __builtin_amdgcn_sched_barrier(0);
                ab_pv(o, va0, pf);
                __builtin_amdgcn_sched_barrier(0);
                ab_qk(n0, n1, Kl + ((t + 1) & 1) * 32768, Ql, kh, rg, r32, hi);
            }
            __builtin_amdgcn_sched_barrier(0);
#pragma unroll
            for (int i = 0; i < 8; ++i) va0[i] ^= 32768u;
            if (t + 2 == nt) ab_mask(n0, n1, dq);
            const float pm = ab_max(n0, n1);
            ab_rescale(pm, m, l, o);
            s0 = n0; s1 = n1;
        }
        {
            WAITV0();
            __syncthreads();
            bf16x8 pf[4];
            ab_exp(s0, s1, m, l, pf);
            ab_pv(o, va0, pf);
            if (nt & 1) { } else {
#pragma unroll
                for (int i = 0; i < 8; ++i) va0[i] ^= 32768u; }
        }
        l = swapsum(l);
        __syncthreads();
        float* mo = (float*)lds;
        float* ml = (float*)(lds + 65536);
        if (kh == 1) {
#pragma unroll
            for (int i = 0; i < 4; ++i)
#pragma unroll
                for (int r = 0; r < 16; ++r) mo[(rg * 64 + i * 16 + r) * 64 + lane] = o[i][r];
            ml[(rg * 2) * 64 + lane] = m; ml[(rg * 2 + 1) * 64 + lane] = l;
        }
        __syncthreads();
        if (kh == 0) {
            const float m2 = ml[(rg * 2) * 64 + lane], l2 = ml[(rg * 2 + 1) * 64 + lane];
            const float mn = fmaxf(m, m2);
            const float a1 = __builtin_amdgcn_exp2f((m - mn) * AB_C2), a2 = __builtin_amdgcn_exp2f((m2 - mn) * AB_C2);
            const float inv = 1.f / (l * a1 + l2 * a2);
            const float f1 = a1 * inv, f2 = a2 * inv;
            u16* dst = ctx + (size_t)(128 * qb + 32 * rg + r32) * 4096 + head * 128;
#pragma unroll
            for (int i = 0; i < 4; ++i)
#pragma unroll
                for (int g = 0; g < 4; ++g) {
                    float v[4];
#pragma unroll
                    for (int e = 0; e < 4; ++e) v[e] = o[i][4 * g + e] * f1 + mo[(rg * 64 + i * 16 + 4 * g + e) * 64 + lane] * f2;
                    u32x2 w = {cvtpk(v[0], v[1]), cvtpk(v[2], v[3])};
                    *(u32x2*)(dst + 32 * i + 8 * g + 4 * hi) = w;
                }
        }
        __syncthreads();
    }
}

constexpr int AA_LDS = 65536 + 69632 + 8 * 64 * 10 * 4;
__global__ __launch_bounds__(1024, 1) void attnA_kernel(const u16* __restrict__ Qa, const u16* __restrict__ Ka,
                                                        const u16* __restrict__ Vimg, u16* __restrict__ ctxA) {
    extern __shared__ __attribute__((aligned(16))) char lds[];
    const int tid = threadIdx.x, lane = tid & 63, wid = __builtin_amdgcn_readfirstlane(tid >> 6);
    const int r32 = lane & 31, hi = lane >> 5, tk = wid & 7, kh = wid >> 3;
#pragma unroll
    for (int i = 0; i < 4; ++i) { int p = tid + 1024 * i; glds16(Ka + p * 8, lds + p * 16); }
#pragma unroll
    for (int i = 0; i < 5; ++i) { int p = tid + 1024 * i; if (p < 4352) glds16(Vimg + p * 8, lds + 65536 + p * 16); }
    const int g = blockIdx.x * 8 + tk, head = g & 31, qblk = g >> 5;
    const int row = 32 * qblk + r32;
    constexpr float C2 = 0.25f * LOG2E;
    bf16x8 qf;
    {
        const u32x4 qr = *(const u32x4*)(Qa + (size_t)row * 512 + head * 16 + 8 * hi);
        u32x4 qs;
#pragma unroll
        for (int i = 0; i < 4; ++i)
            qs[i] = cvtpk(__uint_as_float(qr[i] << 16) * C2, __uint_as_float(qr[i] & 0xffff0000u) * C2);
        qf = __builtin_bit_cast(bf16x8, qs);
    }
    const int dd = r32 <= 16 ? r32 : (r32 & 15);
    const char* kp = lds + kh * 32768 + r32 * 32 + 16 * hi;
    const char* vp = lds + 65536 + kh * (64 * 544) + (dd * 2 + hi) * 16;
    f32x16 o = f32x16{};
    float m = 0.f;
    f32x16 negm = f32x16{};
    WAITV0();
    __syncthreads();
    for (int it = 0; it < 16; ++it) {
        bf16x8 k0 = *(const bf16x8*)(kp + it * 2048);
        bf16x8 k1 = *(const bf16x8*)(kp + it * 2048 + 1024);
        f32x16 s0 = __builtin_amdgcn_mfma_f32_32x32x16_bf16(k0, qf, negm, 0, 0, 0);
        f32x16 s1 = __builtin_amdgcn_mfma_f32_32x32x16_bf16(k1, qf, negm, 0, 0, 0);
        float pm = s0[0];
#pragma unroll
        for (int r = 1; r < 16; ++r) pm = fmaxf(pm, s0[r]);
#pragma unroll
        for (int r = 0; r < 16; ++r) pm = fmaxf(pm, s1[r]);
        pm = swapmax(pm);
        if (it == 0 || !__all(pm <= 8.f)) {
            const float dl = it == 0 ? pm : fmaxf(pm, 0.f);
            const float alpha = it == 0 ? 1.f : __builtin_amdgcn_exp2f(-dl);
            m += dl;
#pragma unroll
            for (int r = 0; r < 16; ++r) { s0[r] -= dl; s1[r] -= dl; negm[r] = -m; }
#pragma unroll
            for (int r = 0; r < 9; ++r) o[r] *= alpha;
        }
#pragma unroll
        for (int r = 0; r < 16; ++r) { s0[r] = __builtin_amdgcn_exp2f(s0[r]); s1[r] = __builtin_amdgcn_exp2f(s1[r]); }
#pragma unroll
        for (int s = 0; s < 2; ++s) {
            u32x4 w0 = {cvtpk(s0[8 * s], s0[8 * s + 1]), cvtpk(s0[8 * s + 2], s0[8 * s + 3]), cvtpk(s0[8 * s + 4], s0[8 * s + 5]), cvtpk(s0[8 * s + 6], s0[8 * s + 7])};
            u32x4 w1 = {cvtpk(s1[8 * s], s1[8 * s + 1]), cvtpk(s1[8 * s + 2], s1[8 * s + 3]), cvtpk(s1[8 * s + 4], s1[8 * s + 5]), cvtpk(s1[8 * s + 6], s1[8 * s + 7])};
            bf16x8 v0 = *(const bf16x8*)(vp + (4 * it + s) * 544);
            bf16x8 v1 = *(const bf16x8*)(vp + (4 * it + 2 + s) * 544);
            o = __builtin_amdgcn_mfma_f32_32x32x16_bf16(v0, __builtin_bit_cast(bf16x8, w0), o, 0, 0, 0);
            o = __builtin_amdgcn_mfma_f32_32x32x16_bf16(v1, __builtin_bit_cast(bf16x8, w1), o, 0, 0, 0);
        }
    }
    float* mg = (float*)(lds + 65536 + 69632) + tk * 640;
    if (kh == 1) {
#pragma unroll
        for (int r = 0; r < 9; ++r) mg[r * 64 + lane] = o[r];
        mg[9 * 64 + lane] = m;
    }
    __syncthreads();
    if (kh == 0) {
        const float m2 = mg[9 * 64 + lane];
        const float mn = fmaxf(m, m2);
        const float a1 = __builtin_amdgcn_exp2f(m - mn), a2 = __builtin_amdgcn_exp2f(m2 - mn);
        float ov[9];
#pragma unroll
        for (int r = 0; r < 9; ++r) ov[r] = o[r] * a1 + mg[r * 64 + lane] * a2;
        const float l = __shfl(ov[8], r32);
        const float inv = 1.f / l;
        u16* dst = ctxA + (size_t)row * 512 + head * 16 + 4 * hi;
#pragma unroll
        for (int g2 = 0; g2 < 2; ++g2) {
            u32x2 w = {cvtpk(ov[4 * g2] * inv, ov[4 * g2 + 1] * inv), cvtpk(ov[4 * g2 + 2] * inv, ov[4 * g2 + 3] * inv)};
            *(u32x2*)(dst + 8 * g2) = w;
        }
    }
}

extern "C" void kernel_launch(void* const* d_in, const int* in_sizes, int n_in,
                              void* d_out, int out_size, void* d_ws, size_t ws_size,
                              hipStream_t stream) {
    const u16* A_x = (const u16*)d_in[0];
    const u16* A_wq = (const u16*)d_in[1];
    const u16* A_wk = (const u16*)d_in[2];
    const u16* A_wv = (const u16*)d_in[3];
    const u16* A_wo = (const u16*)d_in[4];
    const u16* B_x = (const u16*)d_in[5];
    const u16* B_ckv = (const u16*)d_in[6];
    const u16* B_wq = (const u16*)d_in[7];
    const u16* B_wuk = (const u16*)d_in[8];
    const u16* B_wuv = (const u16*)d_in[9];
    const u16* B_wo = (const u16*)d_in[10];
    char* ws = (char*)d_ws;
    const size_t MB = 1 << 20;
    u16* QKV = (u16*)ws;
    u16* ctxB = (u16*)(ws + 48 * MB);
    u16* Qa = (u16*)(ws + 64 * MB);
    u16* ctxA = (u16*)(ws + 66 * MB);
    u16* Ka = (u16*)(ws + 68 * MB);
    u16* Vimg = (u16*)(ws + 68 * MB + 65536);
    float* part = (float*)(ws + 72 * MB);
    u16* out = (u16*)d_out;

    hipFuncSetAttribute((const void*)proj_kernel, hipFuncAttributeMaxDynamicSharedMemorySize, P_LDS);
    hipFuncSetAttribute((const void*)oproj_kernel, hipFuncAttributeMaxDynamicSharedMemorySize, O_LDS);
    hipFuncSetAttribute((const void*)attnB_kernel, hipFuncAttributeMaxDynamicSharedMemorySize, AB_LDS);
    hipFuncSetAttribute((const void*)attnA_kernel, hipFuncAttributeMaxDynamicSharedMemorySize, AA_LDS);

    proj_kernel<<<256, 512, P_LDS, stream>>>(A_x, A_wq, A_wk, A_wv, B_x, B_ckv, B_wq, B_wuk, B_wuv, QKV, Qa, Ka, Vimg);
    attnB_kernel<<<256, 512, AB_LDS, stream>>>(QKV, ctxB);
    attnA_kernel<<<256, 1024, AA_LDS, stream>>>(Qa, Ka, Vimg, ctxA);
    oproj_kernel<<<320, 512, O_LDS, stream>>>(ctxB, B_wo, ctxA, A_wo, part, out);
    reduce_kernel<<<1024, 256, 0, stream>>>(part, out);
}
```
